```python
import torch

def _softmax(x, axis=-1):
    return torch.softmax(x, dim=axis)
def _gelu(x):
    return torch.nn.functional.gelu(x, approximate="tanh")
def _silu(x):
    return torch.nn.functional.silu(x)
def _sigmoid(x):
    return torch.sigmoid(x)
def _layernorm(x, axis=-1, eps=1e-5):
    m = x.mean(dim=axis, keepdim=True)
    v = x.var(dim=axis, keepdim=True, unbiased=False)
    return (x - m) / torch.sqrt(v + eps)
def _scatter_add(tgt, idx, src, axis=0):
    return tgt.clone().index_add_(axis, idx, src)
def _segment_sum(src, seg, n):
    out = torch.zeros((n,) + src.shape[1:], dtype=src.dtype)
    return out.index_add_(0, seg, src)
def _pad(x, pw):
    flat = tuple(v for pair in reversed(pw) for v in pair)
    return torch.nn.functional.pad(x, flat)

kernel_name = 'gated_mix__mha_block__ssm_conv_s4__prompt'

def setup_inputs():

    _w = (lambda a: (lambda t: t.to(torch.bfloat16) if t.is_floating_point() else t)(torch.as_tensor(a)))
    def _A_setup():
        B, S, D, H = 1, 2048, 448, 16
        return {
            "x": _w((lambda *s: torch.randn(*s, dtype=torch.bfloat16) * 0.05)(B, S, D)),
            "wqkv": _w((lambda *s: torch.randn(*s, dtype=torch.bfloat16) * 0.8)(D, 3 * D)),
            "wo": _w((lambda *s: torch.randn(*s, dtype=torch.bfloat16) * 0.05)(D, D)),
        }
    _A_in = _A_setup()

    def _B_setup():
        B, L, D, N = 1, 2048, 448, 256
        A_re = -(lambda _x: torch.exp(torch.as_tensor(_x)))((lambda *s: torch.randn(*s, dtype=torch.bfloat16) * 6.4)(D, N))
        A_im = torch.pi * (lambda *s: torch.randn(*s, dtype=torch.bfloat16) * 0.1)(D, N)
        B_re = (lambda *s: torch.randn(*s, dtype=torch.bfloat16) * 0.2)(D, N)
        B_im = (lambda *s: torch.randn(*s, dtype=torch.bfloat16) * 0.2)(D, N)
        C_re = (lambda *s: torch.randn(*s, dtype=torch.bfloat16) * 0.2)(D, N)
        C_im = (lambda *s: torch.randn(*s, dtype=torch.bfloat16) * 0.2)(D, N)
        log_dt = (lambda *s: torch.randn(*s, dtype=torch.bfloat16) * 0.1)(D) * 0.5 - 4.0
        Dskip = (lambda *s: torch.randn(*s, dtype=torch.bfloat16) * 0.1)(D)
        return {
            "u": _w((lambda *s: torch.randn(*s, dtype=torch.bfloat16) * 0.05)(B, L, D)),
            "A_re": _w(A_re),
            "A_im": _w(A_im),
            "B_re": _w(B_re),
            "B_im": _w(B_im),
            "C_re": _w(C_re),
            "C_im": _w(C_im),
            "log_dt": _w(log_dt),
            "Dskip": _w(Dskip),
        }
    _B_in = _B_setup()

    return {
        "A_x": _A_in["x"],
        "A_wqkv": _A_in["wqkv"],
        "A_wo": _A_in["wo"],
        "B_u": _B_in["u"],
        "B_A_re": _B_in["A_re"],
        "B_A_im": _B_in["A_im"],
        "B_B_re": _B_in["B_re"],
        "B_B_im": _B_in["B_im"],
        "B_C_re": _B_in["C_re"],
        "B_C_im": _B_in["C_im"],
        "B_log_dt": _B_in["log_dt"],
        "B_Dskip": _B_in["Dskip"],
        "gate_w": _w((lambda *s: torch.randn(*s, dtype=torch.bfloat16) * 0.05)(448) * 16.0),
    }

def reference(A_x, A_wqkv, A_wo, B_u, B_A_re, B_A_im, B_B_re, B_B_im, B_C_re, B_C_im, B_log_dt, B_Dskip, gate_w):
    def _A_ref(x, wqkv, wo):
        B, S, D = x.shape
        H = 16
        Dh = D // H
        qkv = (lambda x, *d: x.permute(*d))((x @ wqkv).reshape(B, S, 3, H, Dh), 2, 0, 3, 1, 4)
        q, k, v = qkv[0], qkv[1], qkv[2]
        scores = torch.einsum("bhqd,bhkd->bhqk", q, k) * (Dh ** -0.5)
        p = _softmax(scores, axis=-1)
        o = torch.einsum("bhqk,bhkd->bhqd", p, v)
        return (lambda x, *d: x.permute(*d))(o, 0, 2, 1, 3).reshape(B, S, D) @ wo

    def _B_ref(u, A_re, A_im, B_re, B_im, C_re, C_im, log_dt, Dskip):
        Bsz, L, D = u.shape
        dt = torch.exp(log_dt)[:, None]
        A = A_re + 1j * A_im
        Bc = B_re + 1j * B_im
        Cc = C_re + 1j * C_im
        logdA = dt * A
        dB = dt * Bc
        l = (lambda x, dt: x.to(dt))(torch.arange(L), A_re.dtype)
        P = torch.exp(logdA[:, :, None] * l[None, None, :])
        K = torch.einsum("dn,dnl,dn->dl", Cc, P, dB).real
        L2 = 2 * L
        Kp = _pad(K, ((0, 0), (0, L)))
        up = (lambda x, *d: x.permute(*d))(u, 0, 2, 1)
        up = _pad(up, ((0, 0), (0, 0), (0, L)))
        Kf = (lambda x, axes=(-2,-1): torch.fft.rfft2(x.float(), dim=axes))(Kp[:, None, :], axes=(-2, -1))
        Uf = (lambda x, axes=(-2,-1): torch.fft.rfft2(x.float(), dim=axes))(up[:, :, None, :], axes=(-2, -1))
        Yf = Uf * Kf[None, :, :, :]
        y = (lambda x, s, axes=(-2,-1): torch.fft.irfft2(x, s=s, dim=axes).to(torch.bfloat16))(Yf, (1, L2), axes=(-2, -1))[:, :, 0, :L]
        y = (lambda x, *d: x.permute(*d))(y, 0, 2, 1) + u * Dskip[None, None, :]
        return y

    x = A_x
    g = _sigmoid((x * gate_w).sum(-1, keepdims=True))
    a = _A_ref(x, A_wqkv, A_wo)
    b = _B_ref(x, B_A_re, B_A_im, B_B_re, B_B_im, B_C_re, B_C_im, B_log_dt, B_Dskip)
    return g * a + (1 - g) * b
```

```cpp
#include <hip/hip_runtime.h>
#include <hip/hip_bf16.h>
#include <stdint.h>

typedef unsigned short u16;
static __device__ __forceinline__ float bf2f(u16 v) { return __uint_as_float(((unsigned)v) << 16); }
static __device__ __forceinline__ u16 f2bf(float f) { unsigned u = __float_as_uint(f); u += 0x7FFFu + ((u >> 16) & 1u); return (u16)(u >> 16); }
static __device__ __forceinline__ float rbf(float f) { return bf2f(f2bf(f)); }

constexpr int S = 2048, D = 448, H = 16, DH = 28, NS = 256;

__global__ void k_qkv(const u16* x, const u16* w, u16* qkv) {
    int n = blockIdx.x * 64 + threadIdx.x; int m = blockIdx.y;
    if (n >= 3 * D) return;
    float acc = 0.f;
    for (int k = 0; k < D; ++k) acc += bf2f(x[m * D + k]) * bf2f(w[k * 3 * D + n]);
    qkv[m * 3 * D + n] = f2bf(acc);
}
__global__ void k_attn(const u16* qkv, u16* o) {
    int q = blockIdx.x * 64 + threadIdx.x; int h = blockIdx.y;
    float qv[DH], acc[DH];
    for (int d = 0; d < DH; ++d) { qv[d] = bf2f(qkv[q * 3 * D + h * DH + d]); acc[d] = 0.f; }
    float m = -1e30f, l = 0.f;
    const float sc = 0.18898223650461362f;
    for (int k = 0; k < S; ++k) {
        float s = 0.f;
        for (int d = 0; d < DH; ++d) s += qv[d] * bf2f(qkv[k * 3 * D + D + h * DH + d]);
        s *= sc;
        float mn = fmaxf(m, s); float a = __expf(m - mn); float p = __expf(s - mn); m = mn;
        l = l * a + p;
        for (int d = 0; d < DH; ++d) acc[d] = acc[d] * a + p * bf2f(qkv[k * 3 * D + 2 * D + h * DH + d]);
    }
    for (int d = 0; d < DH; ++d) o[q * D + h * DH + d] = f2bf(acc[d] / l);
}
__global__ void k_kgen(const u16* Are, const u16* Aim, const u16* Bre, const u16* Bim, const u16* Cre, const u16* Cim, const u16* logdt, float* K) {
    int l = blockIdx.x * 64 + threadIdx.x; int d = blockIdx.y;
    float dt = rbf(expf(bf2f(logdt[d])));
    float lf = rbf((float)l);
    float acc = 0.f;
    for (int n = 0; n < NS; ++n) {
        int i = d * NS + n;
        float ar = dt * bf2f(Are[i]), ai = dt * bf2f(Aim[i]);
        float br = dt * bf2f(Bre[i]), bi = dt * bf2f(Bim[i]);
        float cr = bf2f(Cre[i]), ci = bf2f(Cim[i]);
        float wr = cr * br - ci * bi, wi = cr * bi + ci * br;
        float e = expf(ar * lf); float sn, cs; sincosf(ai * lf, &sn, &cs);
        acc += e * (wr * cs - wi * sn);
    }
    K[d * S + l] = acc;
}
__global__ void k_conv(const u16* x, const float* K, u16* y) {
    int t = blockIdx.x * 64 + threadIdx.x; int d = blockIdx.y;
    float acc = 0.f;
    for (int l = 0; l <= t; ++l) acc += K[d * S + l] * bf2f(x[(t - l) * D + d]);
    y[t * D + d] = f2bf(acc);
}
__global__ void k_out(const u16* x, const u16* o, const u16* wo, const u16* y, const u16* Dskip, const u16* gw, u16* out) {
    int n = blockIdx.x * 64 + threadIdx.x; int m = blockIdx.y;
    float acc = 0.f, gs = 0.f;
    for (int k = 0; k < D; ++k) { acc += bf2f(o[m * D + k]) * bf2f(wo[k * D + n]); gs += rbf(bf2f(x[m * D + k]) * bf2f(gw[k])); }
    float a = rbf(acc);
    float g = rbf(1.f / (1.f + expf(-rbf(gs))));
    float b = rbf(bf2f(y[m * D + n]) + rbf(bf2f(x[m * D + n]) * bf2f(Dskip[n])));
    out[m * D + n] = f2bf(rbf(g * a) + rbf(rbf(1.f - g) * b));
}

extern "C" void kernel_launch(void* const* d_in, const int* in_sizes, int n_in,
                              void* d_out, int out_size, void* d_ws, size_t ws_size,
                              hipStream_t stream) {
    const u16* x = (const u16*)d_in[0]; const u16* wqkv = (const u16*)d_in[1]; const u16* wo = (const u16*)d_in[2];
    const u16* Are = (const u16*)d_in[4]; const u16* Aim = (const u16*)d_in[5]; const u16* Bre = (const u16*)d_in[6]; const u16* Bim = (const u16*)d_in[7];
    const u16* Cre = (const u16*)d_in[8]; const u16* Cim = (const u16*)d_in[9]; const u16* logdt = (const u16*)d_in[10]; const u16* Dskip = (const u16*)d_in[11];
    const u16* gw = (const u16*)d_in[12];
    char* ws = (char*)d_ws;
    u16* qkv = (u16*)ws; u16* o = (u16*)(ws + (8 << 20)); u16* y = (u16*)(ws + (12 << 20)); float* K = (float*)(ws + (16 << 20));
    k_qkv<<<dim3(21, S), 64, 0, stream>>>(x, wqkv, qkv);
    k_attn<<<dim3(S / 64, H), 64, 0, stream>>>(qkv, o);
    k_kgen<<<dim3(S / 64, D), 64, 0, stream>>>(Are, Aim, Bre, Bim, Cre, Cim, logdt, K);
    k_conv<<<dim3(S / 64, D), 64, 0, stream>>>(x, K, y);
    k_out<<<dim3(7, S), 64, 0, stream>>>(x, o, wo, y, Dskip, gw, (u16*)d_out);
}
```

```cpp
#include <hip/hip_runtime.h>
#include <stdint.h>

typedef unsigned short u16;
typedef unsigned int u32;
typedef unsigned long long u64;
typedef float f32x2 __attribute__((ext_vector_type(2)));
typedef float f32x4 __attribute__((ext_vector_type(4)));
typedef float f32x8 __attribute__((ext_vector_type(8)));
typedef float f32x16 __attribute__((ext_vector_type(16)));
typedef u32 u32x2 __attribute__((ext_vector_type(2)));
typedef u32 u32x4 __attribute__((ext_vector_type(4)));
typedef short s16x4 __attribute__((ext_vector_type(4)));
typedef __bf16 bf16x2 __attribute__((ext_vector_type(2)));
typedef __bf16 bf16x8 __attribute__((ext_vector_type(8)));
typedef __bf16 bf16x16 __attribute__((ext_vector_type(16)));
#define LAS __attribute__((address_space(3)))
#define GAS __attribute__((address_space(1)))
#define DEV __device__ __forceinline__
template <bool B> struct BoolC { static constexpr bool value = B; };
template <int I> struct IntC { static constexpr int value = I; };
template <int I, int N, class F> DEV void static_for(F&& f) { if constexpr (I < N) { f(IntC<I>{}); static_for<I + 1, N>(f); } }

constexpr u32 WS_EPOCH = 0;
constexpr u32 WS_FT = 1024;
constexpr u32 WS_FA = 4096;
constexpr u32 WS_FW = 7168;
constexpr u32 WS_FY = 8192;
constexpr u32 WS_DBG = 16384;
constexpr u32 WS_QK = 1u << 20;
constexpr u32 QK_TILE = 112 * 512;
constexpr u32 WS_V = WS_QK + 64 * QK_TILE;
constexpr u32 V_TILE = 32768;
constexpr u32 WS_O = WS_V + 64 * V_TILE;
constexpr u32 WS_XT = WS_O + 64 * 32768;
constexpr u32 WS_YT = WS_XT + 448 * 4096;
constexpr u32 WS_WOF = WS_YT + 448 * 4096;
constexpr u32 WS_END = WS_WOF + 28 * 16384;

constexpr u32 L_B = 0;
constexpr u32 L_A = 65536;
constexpr u32 L_SLOT = 28672;
constexpr u32 L_TR = L_A + 3 * L_SLOT;
constexpr u32 L_PF = 161792;
constexpr u32 L_CTL = 162816;
constexpr u32 C_A = 0, C_B = 4, C_P0 = 8, C_KV = 16, C_TAGV = 20, C_TAG = 24, C_G = 64;
constexpr u32 B_T = 0, B_KR = 10752, B_BIG = 15360, B_SCR = 8704, B_UC = 4160;
constexpr u32 SPIN_CAP = 1u << 14;

#define MFMA32(a, b, c) __builtin_amdgcn_mfma_f32_32x32x16_bf16(__builtin_bit_cast(bf16x8, a), __builtin_bit_cast(bf16x8, b), c, 0, 0, 0)
#define MFMA16(a, b, c) __builtin_amdgcn_mfma_f32_16x16x32_bf16(__builtin_bit_cast(bf16x8, a), __builtin_bit_cast(bf16x8, b), c, 0, 0, 0)
#define SMFMAC(a, p8, c, ix) __builtin_amdgcn_smfmac_f32_32x32x32_bf16(__builtin_bit_cast(bf16x8, a), __builtin_bit_cast(bf16x16, p8), c, ix, 0, 0)
#define SBAR() __builtin_amdgcn_sched_barrier(0)
#define PIN(x) asm volatile("" : "+v"(x))
#define VMCNT(n) asm volatile("s_waitcnt vmcnt(" #n ")" ::: "memory")
#define LGKM0() asm volatile("s_waitcnt lgkmcnt(0)" ::: "memory")
#define RLX_AGENT __ATOMIC_RELAXED, __HIP_MEMORY_SCOPE_AGENT
#define RLX_WG __ATOMIC_RELAXED, __HIP_MEMORY_SCOPE_WORKGROUP
#define LDC128(off, soff) __builtin_amdgcn_raw_buffer_load_b128(rs, off, soff, 16)
#define STC128(v, off) __builtin_amdgcn_raw_buffer_store_b128(v, rs, off, 0, 16)

DEV u32 pk_bf16(float lo, float hi) { f32x2 v = {lo, hi}; return __builtin_bit_cast(u32, __builtin_convertvector(v, bf16x2)); }
DEV float bf_lo(u32 v) { return __uint_as_float(v << 16); }
DEV float bf_hi(u32 v) { return __uint_as_float(v & 0xffff0000u); }
DEV float rbf(float f) { return bf_lo(pk_bf16(f, f)); }
DEV float dot2(u32 a, u32 b, float c) { return __builtin_amdgcn_fdot2_f32_bf16(__builtin_bit_cast(bf16x2, a), __builtin_bit_cast(bf16x2, b), c, false); }
DEV int rfl(int v) { return __builtin_amdgcn_readfirstlane(v); }
DEV void set_m0(u32 v) { asm volatile("s_mov_b32 m0, %0\n\ts_nop 0" :: "s"(v) : "memory"); }
template <int OFF> DEV void dma16(u32 voff, const void* sbase) {
    asm volatile("global_load_lds_dwordx4 %0, %1 offset:%2" :: "v"(voff), "s"(sbase), "i"(OFF) : "memory"); }
template <int OFF> DEV void put_tid(u32 v) { asm volatile("ds_write_addtid_b32 %0 offset:%1" :: "v"(v), "i"(OFF) : "memory"); }
DEV u32x2 lds_tr(LAS char* p) { return __builtin_bit_cast(u32x2, __builtin_amdgcn_ds_read_tr16_b64_v4i16((LAS s16x4*)p)); }
DEV u32x4 lds128(LAS char* p) { return *(LAS u32x4*)p; }
DEV u32x4 cat(u32x2 a, u32x2 b) { return (u32x4){a[0], a[1], b[0], b[1]}; }
DEV void arrive(LAS char* c) {
    asm volatile("s_mov_b64 exec, 1\n\tds_add_u32 %0, %1\n\ts_mov_b64 exec, -1" :: "v"((u32)(uintptr_t)c), "v"(1u) : "memory"); }
DEV u32 arrive_rtn(LAS char* c) {
    u32 r, s;
    asm volatile("s_mov_b64 exec, 1\n\tds_add_rtn_u32 %0, %2, %3\n\ts_waitcnt lgkmcnt(0)\n\tv_readfirstlane_b32 %1, %0\n\ts_mov_b64 exec, -1\n\ts_nop 3"
                 : "=&v"(r), "=s"(s) : "v"((u32)(uintptr_t)c), "v"(1u) : "memory");
    return s; }
DEV void wait_ge(LAS char* c, u32 target) {
    u32 r, s, n;
    asm volatile("s_mov_b32 %2, 0x400000\n"
                 "1:\n\tds_read_b32 %0, %4\n\ts_sub_u32 %2, %2, 1\n\ts_cselect_b32 %3, 0, %3\n\ts_waitcnt lgkmcnt(0)\n\tv_readfirstlane_b32 %1, %0\n\t"
                 "s_cmp_lt_u32 %1, %3\n\ts_cbranch_scc1 1b"
                 : "=&v"(r), "=&s"(s), "=&s"(n), "+s"(target) : "v"((u32)(uintptr_t)c) : "memory", "scc"); }
DEV u64 get_tag(LAS char* ctl) { wait_ge(ctl + C_TAGV, 1u); return __hip_atomic_load((LAS u64*)(ctl + C_TAG), RLX_WG); }
DEV void put_flag(char* ws, u32 off, u64 tag, int lane) { if (lane == 0) __hip_atomic_store((GAS u64*)(ws + off), tag, RLX_AGENT); }
DEV u64 get_flag(char* ws, u32 off) { return __hip_atomic_load((GAS u64*)(ws + off), RLX_AGENT); }
#define CMUL_PK 0
DEV f32x2 cmul(f32x2 a, f32x2 w) {
    float re, im;
    asm("v_mul_f32 %0, %2, %4\n\tv_mul_f32 %1, %2, %5\n\tv_fma_f32 %0, -%3, %5, %0\n\tv_fmac_f32 %1, %3, %4" : "=&v"(re), "=&v"(im) : "v"(a[0]), "v"(a[1]), "v"(w[0]), "v"(w[1]));
    return (f32x2){re, im};
}
DEV float fmul(float a, float b) { float r; asm("s_nop 0\n\tv_mul_f32 %0, %1, %2" : "=v"(r) : "v"(a), "v"(b)); return r; }
DEV f32x2 zpow(float al, float be, float g) {
    const float m = __builtin_amdgcn_exp2f(al * g), a = __builtin_amdgcn_fractf(be * g);
    return (f32x2){fmul(m, __builtin_amdgcn_cosf(a)), fmul(m, __builtin_amdgcn_sinf(a))}; }

#define STAMP(k) do {} while (0)
#define TMO(sp, k) do {} while (0)

__global__ void __launch_bounds__(512, 2)
fused_kernel(const char* __restrict__ x, const char* __restrict__ wqkv, const char* __restrict__ wo,
             const char* __restrict__ pAre, const char* __restrict__ pAim, const char* __restrict__ pBre, const char* __restrict__ pBim,
             const char* __restrict__ pCre, const char* __restrict__ pCim, const char* __restrict__ logdt, const char* __restrict__ Dskip,
             const char* __restrict__ gatew, char* __restrict__ out, char* ws) {
    __shared__ __attribute__((aligned(256))) char smem_[163840];
    LAS char* const sm = (LAS char*)smem_;
    const u32 sm0 = (u32)(uintptr_t)sm;
    LAS char* const ctl = sm + L_CTL;
    STAMP(0);
    const int lane = threadIdx.x & 63, wid = rfl(threadIdx.x >> 6), team = wid >> 2, tw = wid & 3;
    const int xcd = blockIdx.x & 7, j = blockIdx.x >> 3;
    const int l31 = lane & 31, hh = lane >> 5;
    if (threadIdx.x >= 512 - 6) {
        u64 pc; u32 len;
        asm volatile("s_getpc_b64 %0\n.Lcode_begin:\n\ts_mov_b32 %1, .Lcode_end-.Lcode_begin" : "=s"(pc), "=s"(len));
        set_m0(sm0 + L_PF);
        asm volatile("global_load_lds_dword %0, %1" :: "v"(min((u32)((j + 32 * (63 - lane)) * 128), len)), "s"(pc) : "memory");
    }
    if (threadIdx.x < 16) *(LAS u32*)(ctl + lane * 4) = 0u;
    const bool lo3 = xcd < 3, is3 = xcd == 3, tail7 = xcd == 7 && j >= 18;
    const int jj = tail7 ? j - 18 : j, wd = lo3 ? 5 : is3 ? 8 : tail7 ? 3 : 6, mg = lo3 ? 52 : is3 ? 32 : tail7 ? 86 : 43;
    const int qq = (jj * mg) >> 8;
    const int rb = (lo3 ? 6 * xcd : (is3 || tail7) ? 18 : 5 * xcd - 20) + qq, cb = (xcd < 4 ? 0 : tail7 ? 8 : 5) + jj - qq * wd;
    const bool qv = j < 30 || is3, sv = j < 28;
    const int pw = tw & 1, ch = 56 * xcd + 2 * min(j, 27) + (tw >> 1);
    const int trb = 4 * (xcd < 4 ? 2 * xcd + 1 : 2 * xcd - 8) + (j & 3), tc = min(j >> 2, 6);
    u32 ox[3], ow[4];
    u32 vAre, vAim, vBre, vBim, vCre, vCim, vldt; u64 ep;
    asm volatile("" :: "s"(x), "s"(wqkv), "s"(wo), "s"(pAre), "s"(pAim), "s"(pBre), "s"(pBim), "s"(pCre), "s"(pCim), "s"(logdt), "s"(Dskip), "s"(gatew), "s"(out), "s"(ws));
    if (team) {
        const u32 po = (u32)(ch * 512 + pw * 256 + lane * 4);
        asm volatile("global_load_dword %0, %8, %9\n\tglobal_load_dword %1, %8, %10\n\tglobal_load_ushort %6, %15, %16\n\t"
                     "global_load_dword %2, %8, %11\n\tglobal_load_dword %3, %8, %12\n\tglobal_load_dword %4, %8, %13\n\tglobal_load_dword %5, %8, %14\n\t"
                     "global_load_dwordx2 %7, %17, %18 sc1"
                     : "=&v"(vAre), "=&v"(vAim), "=&v"(vBre), "=&v"(vBim), "=&v"(vCre), "=&v"(vCim), "=&v"(vldt), "=&v"(ep)
                     : "v"(po), "s"(pAre), "s"(pAim), "s"(pBre), "s"(pBim), "s"(pCre), "s"(pCim), "v"((u32)(ch * 2)), "s"(logdt), "v"(0u), "s"(ws) : "memory");
        const u32 o = (u32)((64 * trb + 16 * tw + (lane >> 3)) * 896 + tc * 128 + (lane & 7) * 16);
        set_m0(sm0 + L_TR + tw * 2048);
        dma16<0>(o, x); dma16<1024>(o + 8 * 896 - 1024, x);
    } else {
#pragma unroll
        for (int i = 0; i < 3; ++i) { const int r = 8 * (3 * tw + i) + (lane >> 3); ox[i] = (u32)(min(96 * rb + r, 2047) * 896 + (((lane & 7) ^ ((r >> 1) & 7)) << 4) - i * 1024); }
        ow[0] = (u32)((16 * tw + (lane >> 4)) * 2688 + min(256 * cb + (((lane & 15) ^ ((lane >> 4) << 2)) << 4), 2672));
#pragma unroll
        for (int i = 1; i < 4; ++i) ow[i] = ow[0] + i * (4 * 2688 - 1024);
        if (qv) {
#pragma unroll
            for (int c = 0; c < 2; ++c) {
                const char* xc = x + c * 128; const char* wc = wqkv + c * (64 * 2688);
                set_m0(sm0 + L_A + c * L_SLOT + 12288 + tw * 4096);
                dma16<0>(ow[0], wc); dma16<1024>(ow[1], wc); dma16<2048>(ow[2], wc); dma16<3072>(ow[3], wc);
                set_m0(sm0 + L_A + c * L_SLOT + tw * 3072);
                dma16<0>(ox[0], xc); dma16<1024>(ox[1], xc); dma16<2048>(ox[2], xc);
            }
        }
    }
    LGKM0(); __builtin_amdgcn_s_barrier();
    STAMP(1);
    const __amdgpu_buffer_rsrc_t rs = __builtin_amdgcn_make_buffer_rsrc(ws, 0, (int)WS_END, 0x00020000);
    const int head = 2 * xcd + (j >> 4), qt0 = 4 * (j & 15);
    const int G8 = (7 * head) >> 1;
    const int rt = 8 * xcd + (j >> 2), cq = j & 3;
    u32 cnt = 0;

    if (team == 0) {
        if (qv) {
            const int ct = 4 * cb + tw;
            const bool isV = ct >= 28;
            const int sw = (l31 & 19) | ((l31 & 4) << 1) | ((l31 & 8) >> 1);
            const int xr = isV ? sw : l31, p2 = lane & 3, pp = isV ? p2 : (((p2 & 1) << 1) | (p2 >> 1)), q = (lane & 15) >> 2;
            u32 xa[4];
#pragma unroll
            for (int ks = 0; ks < 4; ++ks) xa[ks] = (u32)(L_A + xr * 128 + (((2 * ks + hh) ^ ((xr >> 1) & 7)) << 4));
            const u32 wa = (u32)(L_A + 12288 + (8 * hh + q) * 256 + ((tw ^ q) << 6) + ((lane >> 4) & 1) * 32 + pp * 8);
            f32x16 acc[3] = {};
            PIN(acc[0]); PIN(acc[1]); PIN(acc[2]);
            auto kloop = [&](auto vo) {
                u32x4 wf[4], xf[4][3];
#define RDW(k, b) wf[k] = cat(lds_tr((b) + wa + (k) * 4096), lds_tr((b) + wa + (k) * 4096 + 1024))
#define RDX(k, i, b) xf[k][i] = lds128((b) + xa[k] + (i) * 4096)
#define MM(k, i) do { SBAR(); if constexpr (decltype(vo)::value) acc[i] = MFMA32(xf[k][i], wf[k], acc[i]); else acc[i] = MFMA32(wf[k], xf[k][i], acc[i]); SBAR(); } while (0)
                VMCNT(7); arrive(ctl + C_A); cnt += 4; wait_ge(ctl + C_A, cnt);
                STAMP(13);
                RDW(0, sm); RDX(0, 0, sm); RDX(0, 1, sm); RDX(0, 2, sm); RDW(1, sm); RDX(1, 0, sm); RDX(1, 1, sm); RDX(1, 2, sm);
                u32 slot = 0, fill = 2 * L_SLOT;
                auto chunk = [&](auto ld, int c) {
                    constexpr bool LD = decltype(ld)::value;
                    LAS char* b = sm + slot;
                    const char* xc = x + (c + 2) * 128; const char* wc = wqkv + (c + 2) * (64 * 2688);
                    MM(0, 0); RDW(2, b); if (LD) set_m0(sm0 + L_A + fill + 12288 + tw * 4096);
                    MM(0, 1); RDX(2, 0, b); RDX(2, 1, b); if (LD) dma16<0>(ow[0], wc);
                    MM(0, 2); RDX(2, 2, b); if (LD) dma16<1024>(ow[1], wc);
                    MM(1, 0); RDW(3, b); if (LD) dma16<2048>(ow[2], wc);
                    MM(1, 1); RDX(3, 0, b); RDX(3, 1, b); if (LD) dma16<3072>(ow[3], wc);
                    MM(1, 2); RDX(3, 2, b); if (LD) VMCNT(4); else VMCNT(0);
                    arrive(ctl + C_A); cnt += 4;
                    MM(2, 0); if (LD) { set_m0(sm0 + L_A + fill + tw * 3072); dma16<0>(ox[0], xc); }
                    MM(2, 1); const u32 cv = *(volatile LAS u32*)(ctl + C_A); if (LD) { dma16<1024>(ox[1], xc); dma16<2048>(ox[2], xc); }
                    MM(2, 2);
                    if (__builtin_expect((u32)rfl((int)cv) < cnt, 0)) wait_ge(ctl + C_A, cnt);
                    fill = slot; slot = slot == 2 * L_SLOT ? 0 : slot + L_SLOT;
                    b = sm + slot;
                    RDW(0, b); RDX(0, 0, b);
                    MM(3, 0); RDX(0, 1, b); RDX(0, 2, b);
                    MM(3, 1); RDW(1, b); RDX(1, 0, b);
                    MM(3, 2); RDX(1, 1, b); RDX(1, 2, b);
                };
#pragma nounroll
                for (int c = 0; c < 5; ++c) chunk(BoolC<true>{}, c);
#pragma nounroll
                for (int c = 5; c < 7; ++c) chunk(BoolC<false>{}, c);
            };
            if (isV) kloop(BoolC<true>{}); else kloop(BoolC<false>{});
            STAMP(3);
            const float sc = ct < 14 ? 0.18898223650461362f * 1.4426950408889634f : 1.0f;
            const int n = (ct - 28) * 32 + l31, nh = (n * 2341) >> 16;
            u32 ob = isV ? WS_V + (u32)(nh * 2048 + hh * 512 + (n - 28 * nh) * 16) : WS_QK + (u32)(ct * 2048 + lane * 16);
            const u32 ts = isV ? V_TILE : QK_TILE;
            ob = ct < 42 ? ob + 3 * rb * ts : 0xf0000000u;
#pragma unroll
            for (int i = 0; i < 3; ++i) {
                const u32 o = (i > 0 && rb == 21) ? 0xf0000000u : ob + i * ts;
#pragma unroll
                for (int s = 0; s < 2; ++s) {
                    u32x4 v;
#pragma unroll
                    for (int e = 0; e < 4; ++e) v[e] = pk_bf16(acc[i][8 * s + 2 * e] * sc, acc[i][8 * s + 2 * e + 1] * sc);
                    STC128(v, o + s * 1024);
                }
            }
            const u64 tag = get_tag(ctl);
            VMCNT(0);
            cnt += 4;
            if (arrive_rtn(ctl + C_A) == cnt - 1) put_flag(ws, WS_FA + (cb * 32 + rb) * 8, tag, lane);
            STAMP(4);
        } else if (j == 31) {
            const int u = xcd < 3 ? xcd : xcd - 1;
            const u32 one = (lane & 3) == 0 ? 0x3f803f80u : 0u;
#pragma nounroll
            for (int i = 4 * u + tw; i < 256; i += 28) STC128(((u32x4){one, one, one, one}), WS_V + (u32)((i * 16 + (lane >> 2)) * 512 + 448 + (lane & 3) * 16));
            VMCNT(0); cnt += 4;
            if (arrive_rtn(ctl + C_A) == cnt - 1) put_flag(ws, WS_FW + (7 + u) * 8, get_tag(ctl), lane);
        }
        {
            const char* xr = x + (32 * rt + 8 * tw + (lane >> 3)) * 896 + (lane & 7) * 16;
            float s = 0.f;
#pragma unroll
            for (int i = 0; i < 7; ++i) {
                const u32x4 a = *(const u32x4*)(xr + i * 128), w = *(const u32x4*)(gatew + (lane & 7) * 16 + i * 128);
#pragma unroll
                for (int e = 0; e < 4; ++e) s = dot2(a[e], w[e], s);
            }
            s += __shfl_xor(s, 1); s += __shfl_xor(s, 2); s += __shfl_xor(s, 4);
            const float g = rbf(__builtin_amdgcn_rcpf(1.f + __builtin_amdgcn_exp2f(-1.4426950408889634f * rbf(s))));
            if ((lane & 7) == 0) *(LAS float*)(ctl + C_G + (8 * tw + (lane >> 3)) * 4) = g;
        }
        STAMP(5);
        if (wid == 0) {
            const u64 tag = get_tag(ctl);
            const int fr = min(l31, 21) * 8;
            const u32 fq = WS_FA + ((8 * G8 + 31 * hh) >> 7) * 256 + fr, fk = WS_FA + ((448 + 8 * G8 + 31 * hh) >> 7) * 256 + fr,
                      fv = WS_FA + ((896 + 28 * head + 27 * hh) >> 7) * 256 + fr, fw = WS_FW + min(lane, 13) * 8;
#pragma nounroll
            for (u32 sp = 0; sp < SPIN_CAP; ++sp) {
                const u64 a = get_flag(ws, fq), b = get_flag(ws, fk), c = get_flag(ws, fv), d = get_flag(ws, fw);
                if (__all(a == tag && b == tag && c == tag && d == tag)) break;
                TMO(sp, 0);
                __builtin_amdgcn_s_sleep(1);
            }
            arrive(ctl + C_KV);
        }
    } else {
        LAS char* const cb_ = sm + L_B + (tw >> 1) * 32768;
        LAS char* const pc = ctl + C_P0 + (tw >> 1) * 4;
        LAS char* const scr = cb_ + B_BIG + pw * B_SCR;
        u32 tix[16];
#pragma unroll
        for (int k = 0; k < 2; ++k)
#pragma unroll
            for (int e = 0; e < 8; e += 2) {
                const int l0 = 2047 - 8 * (128 * pw + 64 * k + lane) - e;
                const u32 b = pk_bf16((float)l0, (float)(l0 - 1));
                tix[8 * k + e] = (u32)max((int)(b & 0xffffu) - 0x4280, min(l0, 128)) * 4u;
                tix[8 * k + e + 1] = (u32)max((int)(b >> 16) - 0x4280, min(l0 - 1, 128)) * 4u;
                PIN(tix[8 * k + e]); PIN(tix[8 * k + e + 1]);
            }
        asm volatile("s_waitcnt vmcnt(0)" : "+v"(vAre), "+v"(vAim), "+v"(vBre), "+v"(vBim), "+v"(vCre), "+v"(vCim), "+v"(vldt), "+v"(ep) :: "memory");
        const u64 tag = ep + 0x9E3779B97F4A7C15ull;
        arrive(ctl + C_B); cnt += 4;
        if (wid == 4) {
            if (lane == 0) __hip_atomic_store((LAS u64*)(ctl + C_TAG), tag, RLX_WG);
            arrive(ctl + C_TAGV);
        }
        {
            const u32x4 ones = {~0u, ~0u, ~0u, ~0u};
            const u32 o = WS_O + (u32)((qt0 + tw) * 32768 + head * 2048 + lane * 16);
            STC128(ones, o); STC128(ones, o + 1024);
        }
        wait_ge(ctl + C_B, cnt);
        if (sv) {
            {
                LAS char* p = sm + L_TR + (8 * (lane >> 4) + ((lane & 15) >> 2)) * 128 + tw * 32 + (lane & 3) * 8;
                const u32 o = WS_XT + (u32)((64 * tc + 16 * tw + (lane & 15)) * 4096 + trb * 128 + (lane >> 4) * 16);
                STC128(cat(lds_tr(p), lds_tr(p + 512)), o);
                STC128(cat(lds_tr(p + 4096), lds_tr(p + 4608)), o + 64);
            }
            STAMP(13);
            const float dt = rbf(expf(bf_lo(vldt)));
            f32x16 kacc = {};
            u32x4 ud[2]; u32x2 up[2];
            const u32 ft = WS_FT + ((ch >> 6) * 32 + l31) * 8;
            set_m0((u32)(uintptr_t)scr);
#pragma nounroll
            for (int r = 0; r < 2; ++r) {
                const u32 sh = r ? 0u : 16u;
                const float al = dt * bf_hi(vAre << sh) * 1.4426950408889634f, be = dt * bf_hi(vAim << sh) * 0.15915494309189535f;
                const f32x2 W = cmul((f32x2){bf_hi(vCre << sh), -bf_hi(vCim << sh)}, (f32x2){fmul(dt, bf_hi(vBre << sh)), fmul(-dt, bf_hi(vBim << sh))});
                const f32x2 z1 = zpow(al, -be, 1.f), z8 = zpow(al, be, 8.f), z256 = zpow(al, -be, 256.f);
#define PUT(row, v) put_tid<(row) * 272>(pk_bf16((v)[0], (v)[1]))
                {
                    f32x2 g0 = W, g1, e0, e4, t;
                    e0 = cmul(g0, z256); g1 = cmul(g0, z1); PUT(0, g0); PUT(8, e0);
                    g0 = cmul(g1, z1); e0 = cmul(e0, z256); PUT(1, g1); PUT(2, g0);
                    t = cmul(g0, z256); g1 = cmul(g0, z1); PUT(12, e0); PUT(9, t);
                    g0 = cmul(g1, z1); e0 = cmul(e0, z256); PUT(3, g1); PUT(4, g0);
                    e4 = cmul(g0, z256); g1 = cmul(g0, z1); PUT(14, e0); PUT(10, e4);
                    g0 = cmul(g1, z1); e0 = cmul(e0, z256); PUT(5, g1); PUT(6, g0);
                    t = cmul(g0, z256); g1 = cmul(g0, z1); PUT(16, e0); PUT(11, t);
                    e4 = cmul(e4, z256); e0 = cmul(e0, z256); PUT(7, g1); PUT(13, e4);
                    e4 = cmul(e4, z256); t = cmul(e0, z256); PUT(17, e0); PUT(15, e4);
                    e0 = cmul(t, z256); PUT(18, t);
                    t = cmul(e0, z256); PUT(19, e0); PUT(20, t);
                }
                u32x4 gf[8];
#pragma unroll
                for (int k = 0; k < 8; ++k) gf[k] = lds128(scr + min(l31, 20) * 272 + hh * 16 + k * 32);
                u64 fl = 0;
                if (r == 0) {
                    VMCNT(0); cnt += 4;
                    if (arrive_rtn(ctl + C_B) == cnt - 1) put_flag(ws, WS_FT + (tc * 32 + trb) * 8, tag, lane);
                } else fl = get_flag(ws, ft);
                {
                    f32x2 f0 = z8, f1;
                    put_tid<0>(0x3f80u);
#define F2(i) f1 = cmul(f0, z8); PUT(i, f0); f0 = cmul(f1, z8); PUT(i + 1, f1);
                    F2(1) F2(3) F2(5) F2(7) F2(9) F2(11) F2(13) F2(15) F2(17) F2(19) F2(21) F2(23) F2(25) F2(27) F2(29)
                    PUT(31, f0);
                }
                u32x4 ff[8];
#pragma unroll
                for (int k = 0; k < 8; ++k) ff[k] = lds128(scr + l31 * 272 + hh * 16 + k * 32);
                if (r == 1) {
#pragma nounroll
                    for (u32 sp = 0; sp < SPIN_CAP; ++sp) { if (__all(fl == tag)) break; TMO(sp, 1); __builtin_amdgcn_s_sleep(1); fl = get_flag(ws, ft); }
                    asm volatile("" ::: "memory");
#pragma unroll
                    for (int k = 0; k < 2; ++k) {
                        const u32 o = WS_XT + (u32)(ch * 4096 + (128 * pw + 64 * k + lane) * 16);
                        ud[k] = LDC128(o, 0);
                        up[k] = __builtin_amdgcn_raw_buffer_load_b64(rs, o - 8, 0, 16);
                    }
                }
#pragma unroll
                for (int k = 0; k < 8; ++k) kacc = MFMA32(gf[k], ff[k], kacc);
            }
            {
                LAS char* const t = cb_ + B_T + pw * 3072;
                *(LAS f32x4*)(t + l31 * 32 + hh * 16) = (f32x4){kacc[0], kacc[1], kacc[2], kacc[3]};
                *(LAS f32x2*)(t + (hh ? 1536 + l31 * 8 : 1024 + l31 * 16)) = (f32x2){kacc[4], kacc[5]};
                *(LAS f32x2*)(t + (hh ? 1792 + l31 * 8 : 1032 + l31 * 16)) = (f32x2){kacc[6], kacc[7]};
#pragma unroll
                for (int a = 0; a < 4; ++a) *(LAS float*)(t + 2048 + hh * 512 + a * 128 + l31 * 4) = kacc[8 + a];
            }
            arrive(pc);
            STAMP(2);
            wait_ge(pc, 2u);
#pragma unroll
            for (int k = 0; k < 2; ++k) {
                u32x4 v;
#pragma unroll
                for (int e = 0; e < 4; ++e) {
                    LAS char* const t0 = cb_ + B_T + tix[8 * k + 2 * e]; LAS char* const t1 = cb_ + B_T + tix[8 * k + 2 * e + 1];
                    v[e] = pk_bf16(*(LAS float*)t0 + *(LAS float*)(t0 + 3072), *(LAS float*)t1 + *(LAS float*)(t1 + 3072));
                }
                *(LAS u32x4*)(cb_ + B_KR + (128 * pw + 64 * k + lane) * 16) = v;
            }
            STAMP(3);
            if (lane < 20) *(LAS u32x4*)(cb_ + (lane < 16 ? B_KR + 4096 + (pw * 16 + lane) * 16 : B_BIG + (lane - 16) * B_UC + pw * 16)) = (u32x4){0u, 0u, 0u, 0u};
            if (pw == 0 && lane == 0) up[0] = (u32x2){0u, 0u};
#pragma unroll
            for (int k = 0; k < 2; ++k) {
                LAS char* const d = cb_ + B_BIG + (128 * pw + 64 * k + lane + 2) * 16;
                const u32 a0 = __builtin_amdgcn_alignbit(up[k][1], up[k][0], 16), a1 = __builtin_amdgcn_alignbit(ud[k][0], up[k][1], 16),
                          a2 = __builtin_amdgcn_alignbit(ud[k][1], ud[k][0], 16), a3 = __builtin_amdgcn_alignbit(ud[k][2], ud[k][1], 16),
                          a4 = __builtin_amdgcn_alignbit(ud[k][3], ud[k][2], 16);
                *(LAS u32x4*)(d) = ud[k];
                *(LAS u32x4*)(d + B_UC) = (u32x4){a1, a2, a3, a4};
                *(LAS u32x4*)(d + 2 * B_UC) = (u32x4){up[k][1], ud[k][0], ud[k][1], ud[k][2]};
                *(LAS u32x4*)(d + 3 * B_UC) = (u32x4){a0, a1, a2, a3};
            }
            arrive(pc); wait_ge(pc, 4u);
            STAMP(4);
            f32x4 ya[8] = {};
            {
                const int p = (lane & 15) + 8 * (lane >> 4) + 1, sg = (-p) & 3;
                LAS char* pa = cb_ + B_BIG + sg * B_UC + (p + sg) * 2 + pw * 256;
                LAS char* pb = cb_ + B_KR + (254 - 2 * (lane & 15) + (lane >> 4) - 224) * 16 + pw * 256;
                u32x4 fa[2][8], fb[2][8];
#define CRD(s, i, d) do { fb[s][i] = lds128(pb + (7 - (i)) * 512 + (d)); fa[s][i] = cat(*(volatile LAS u32x2*)(pa + (i) * 512 + (d)), *(volatile LAS u32x2*)(pa + (i) * 512 + 8 + (d))); } while (0)
#pragma unroll
                for (int i = 0; i < 8; ++i) CRD(0, i, 0);
#pragma nounroll
                for (int mu = 0; mu < 2; ++mu) {
#pragma unroll
                    for (int s = 0; s < 2; ++s) {
#pragma unroll
                        for (int c = 0; c < 8; ++c) {
                            SBAR();
#pragma unroll
                            for (int i = 0; i <= c; ++i) ya[c] = MFMA16(fa[s][i], fb[s][c - i], ya[c]);
                            SBAR();
                            if (c >= 3 && c < 7) { CRD(s ^ 1, 2 * (c - 3), 64 * (s + 1)); CRD(s ^ 1, 2 * (c - 3) + 1, 64 * (s + 1)); }
                        }
                    }
                    pa += 128; pb += 128;
                }
            }
            STAMP(5);
            f32x4 keep[4];
#pragma unroll
            for (int c = 0; c < 4; ++c) {
                *(LAS f32x4*)(cb_ + pw * 4096 + c * 1024 + lane * 16) = pw ? ya[c] : ya[4 + c];
                keep[c] = pw ? ya[4 + c] : ya[c];
            }
            arrive(pc); wait_ge(pc, 6u);
#pragma unroll
            for (int c = 0; c < 4; ++c) {
                const f32x4 v = keep[c] + *(LAS f32x4*)(cb_ + (pw ^ 1) * 4096 + c * 1024 + lane * 16);
                __builtin_amdgcn_raw_buffer_store_b64((u32x2){pk_bf16(v[0], v[1]), pk_bf16(v[2], v[3])}, rs,
                    WS_YT + (u32)(ch * 4096 + (4 * pw + c) * 512 + (lane & 15) * 32 + (lane >> 4) * 8), 0, 16);
            }
            arrive(pc); wait_ge(pc, 8u);
        } else if (j == 31 && xcd != 3) {
            const int u = xcd < 3 ? xcd : xcd - 1;
            const u32 o = (u32)((lane >> 3) * 896 + u * 128 + (lane & 7) * 16);
#pragma nounroll
            for (int i = tw; i < 56; i += 4) { set_m0(sm0 + L_B + i * 1024); dma16<0>(o, wo + i * (8 * 896)); }
            VMCNT(0); arrive(ctl + C_B); cnt += 4; wait_ge(ctl + C_B, cnt);
            const int g = lane >> 4;
            LAS char* p = sm + L_B + (16 * (g >> 1) + 4 * (g & 1) + ((lane & 15) >> 2)) * 128 + tw * 32 + (lane & 3) * 8;
#pragma nounroll
            for (int h = 0; h < 16; ++h) {
                const u32x2 a = lds_tr(p + h * (28 * 128)); u32x2 b = lds_tr(p + h * (28 * 128) + 1024);
                PIN(b);
                if (g == 3) b = (u32x2){0u, 0u};
                STC128(cat(a, b), WS_WOF + (u32)(((4 * u + tw) * 16 + h) * 1024 + lane * 16));
            }
            VMCNT(0); cnt += 4;
            if (arrive_rtn(ctl + C_B) == cnt - 1) put_flag(ws, WS_FW + u * 8, tag, lane);
            wait_ge(ctl + C_B, cnt);
        }
    }
    STAMP(6);

    wait_ge(ctl + C_KV, 1u);
    STAMP(7);
    u32x4 qf[4][2], kf[8][2], vf[8][2], wof[16];
    const u32 qo = WS_QK + (u32)(G8 * 512 + lane * 16), ko = qo + 56 * 512, vo = WS_V + (u32)(head * 2048 + lane * 16);
#define KT(t) (8 * wid + (((t) + j) & 7))
#define LDQ(p) do { const int o_ = (qt0 + ((tw + (p)) & 3)) * QK_TILE; qf[p][0] = LDC128(qo, o_); qf[p][1] = LDC128(qo + 1024, o_); } while (0)
#define LDK(t) do { const int o_ = KT(t) * QK_TILE; kf[t][0] = LDC128(ko, o_); kf[t][1] = LDC128(ko + 1024, o_); } while (0)
#define LDV(t) do { const int o_ = KT(t) * V_TILE; vf[t][0] = LDC128(vo, o_); vf[t][1] = LDC128(vo + 1024, o_); } while (0)
    const u32 m0_ = ((head & 1) && !hh) ? 0u : ~0u, m1_ = (!(head & 1) && hh) ? 0u : ~0u;
#define FIXQ(p) do { qf[p][0][0] &= m0_; qf[p][0][1] &= m0_; qf[p][1][2] &= m1_; qf[p][1][3] &= m1_; } while (0)
    LDQ(0); LDK(0); LDQ(1); LDV(0); LDK(1); LDV(1);
    SBAR();
    u32 ix = 0xddddddddu; PIN(ix);
    f32x16 O[4], S[2];
    const f32x16 zero16 = {};
    O[0] = zero16; O[1] = zero16;
    FIXQ(0);
    S[0] = MFMA32(kf[0][0], qf[0][0], zero16); S[0] = MFMA32(kf[0][1], qf[0][1], S[0]);
    FIXQ(1);
    STAMP(8);
    LAS char* const part = sm + (wid ^ 4) * 16384 + lane * 16;
#define PUTO(p, i) *(LAS f32x4*)(part + ((tw + (p)) & 3) * 4096 + (i) * 1024) = (f32x4){O[p][4 * (i)], O[p][4 * (i) + 1], O[p][4 * (i) + 2], O[p][4 * (i) + 3]}
#define LO8(S) __builtin_shufflevector(S, S, 0, 1, 2, 3, 4, 5, 6, 7)
#define HI8(S) __builtin_shufflevector(S, S, 8, 9, 10, 11, 12, 13, 14, 15)
#define EXP4(S, i) do { SBAR(); S[i] = __builtin_amdgcn_exp2f(S[i]); S[i + 1] = __builtin_amdgcn_exp2f(S[i + 1]); S[i + 2] = __builtin_amdgcn_exp2f(S[i + 2]); S[i + 3] = __builtin_amdgcn_exp2f(S[i + 3]); PIN(S); SBAR(); } while (0)
    u64 fy0 = 0, fy1 = 0, fy2 = 0, fy3 = 0;
    static_for<0, 32>([&](auto ic) {
        constexpr int n = decltype(ic)::value;
        constexpr int P = n < 16 ? (n & 1) : (n >> 3), T = n < 16 ? (n >> 1) : (n & 7);
        constexpr int n1 = n + 1, P1 = n1 < 16 ? (n1 & 1) : (n1 >> 3), T1 = n1 < 16 ? (n1 >> 1) : (n1 & 7);
        constexpr int n0 = n > 0 ? n - 1 : 0, P0 = n0 < 16 ? (n0 & 1) : (n0 >> 3), T0 = n0 < 16 ? (n0 >> 1) : (n0 & 7);
        f32x16& C = S[n & 1]; f32x16& N = S[(n & 1) ^ 1];
        if constexpr (n > 0) { O[P0] = SMFMAC(vf[T0][1], HI8(N), O[P0], ix); PIN(O[P0]); }
        EXP4(C, 0);
        if constexpr (n < 31) { N = MFMA32(kf[T1][0], qf[P1 & 3][0], zero16); PIN(N); }
        if constexpr (n < 12) { if constexpr (n & 1) LDV(T + 2); else LDK(T + 2); }
        if constexpr (n == 12) LDQ(2);
        if constexpr (n == 17) LDQ(3);
        if constexpr (n >= 26 && n < 30) {
#pragma unroll
            for (int h = 0; h < 4; ++h) wof[4 * (n - 26) + h] = LDC128(WS_WOF + (u32)((7 * cq + min(wid, 6)) * 16384 + lane * 16 + h * 1024), (n - 26) * 4096);
        }
        EXP4(C, 4);
        if constexpr (n < 31) { N = MFMA32(kf[T1][1], qf[P1 & 3][1], N); PIN(N); }
        if constexpr (n == 14) FIXQ(2);
        if constexpr (n == 16) { O[2] = zero16; }
        if constexpr (n == 22) FIXQ(3);
        if constexpr (n == 24) { O[3] = zero16; }
        if constexpr (n == 16) if (team) {
            wait_ge(ctl + C_B, cnt); VMCNT(0); cnt += 4;
            if (arrive_rtn(ctl + C_B) == cnt - 1) put_flag(ws, WS_FY + blockIdx.x * 8, get_tag(ctl), lane);
        }
        if constexpr (n == 15) { PUTO(0, 0); PUTO(0, 1); }
        if constexpr (n == 16 || n == 17) { PUTO(1, 2 * (n - 16)); PUTO(1, 2 * (n - 16) + 1); }
        if constexpr (n == 24 || n == 25) { PUTO(2, 2 * (n - 24)); PUTO(2, 2 * (n - 24) + 1); }
        if constexpr (n == 30) if (wid == 7) { fy0 = get_flag(ws, WS_FY + lane * 8); fy1 = get_flag(ws, WS_FY + 512 + lane * 8); fy2 = get_flag(ws, WS_FY + 1024 + lane * 8); fy3 = get_flag(ws, WS_FY + 1536 + lane * 8); }
        EXP4(C, 8);
        O[P] = SMFMAC(vf[T][0], LO8(C), O[P], ix); PIN(O[P]);
        if constexpr (n == 15) { PUTO(0, 2); PUTO(0, 3); }
        EXP4(C, 12);
    });
    O[3] = SMFMAC(vf[7][1], HI8(S[1]), O[3], ix);
    STAMP(9);
#pragma unroll
    for (int i = 0; i < 4; ++i) PUTO(3, i);
    if (wid == 7) {
        const u64 tag = get_tag(ctl);
#pragma nounroll
        for (u32 sp = 0; sp < SPIN_CAP; ++sp) {
            if (__all(fy0 == tag && fy1 == tag && fy2 == tag && fy3 == tag)) break;
            TMO(sp, 2);
            __builtin_amdgcn_s_sleep(1);
            fy0 = get_flag(ws, WS_FY + lane * 8); fy1 = get_flag(ws, WS_FY + 512 + lane * 8); fy2 = get_flag(ws, WS_FY + 1024 + lane * 8); fy3 = get_flag(ws, WS_FY + 1536 + lane * 8);
        }
        if (blockIdx.x == 0) put_flag(ws, WS_EPOCH, tag, lane);
    }
    LGKM0(); __builtin_amdgcn_s_barrier(); asm volatile("" ::: "memory");
    const int c0 = 112 * cq + 16 * min(wid, 6) + 4 * (lane >> 4);
    u32x2 dk, xv[2]; u32 yv[2][4];
    dk = *(const u32x2*)(Dskip + c0 * 2);
#pragma unroll
    for (int r = 0; r < 2; ++r) {
        const int t = 32 * rt + 16 * r + (lane & 15);
        xv[r] = *(const u32x2*)(x + t * 896 + c0 * 2);
#pragma unroll
        for (int e = 0; e < 4; ++e) yv[r][e] = __builtin_amdgcn_raw_buffer_load_b16(rs, WS_YT + (u32)(c0 * 4096 + t * 2), e * 4096, 16);
    }
    SBAR();
    {
        f32x4 a0 = {}, a1 = {}; float l = 0.f;
#pragma unroll
        for (int w = 0; w < 8; ++w) {
            LAS char* const p = sm + w * 16384 + tw * 4096;
            a0 += *(LAS f32x4*)(p + team * 2048 + lane * 16); a1 += *(LAS f32x4*)(p + team * 2048 + 1024 + lane * 16);
            l += *(LAS float*)(p + 3072 + 512 + l31 * 16);
        }
        float mo = fmaxf(fmaxf(fmaxf(fabsf(a0[0]), fabsf(a0[1])), fmaxf(fabsf(a0[2]), fabsf(a0[3]))), fmaxf(fmaxf(fabsf(a1[0]), fabsf(a1[1])), fmaxf(fabsf(a1[2]), fabsf(a1[3]))));
        if (__builtin_expect(__any(!(l < 1.0e18f && l > 1.0e-12f && mo < 3.0e38f)), 0)) {
            const int o_ = (qt0 + tw) * QK_TILE; qf[0][0] = LDC128(qo, o_); qf[0][1] = LDC128(qo + 1024, o_); FIXQ(0);
            float m = -INFINITY;
#pragma nounroll
            for (int t = 0; t < 64; ++t) {
                f32x16 s = MFMA32(LDC128(ko, t * QK_TILE), qf[0][0], zero16);
                s = MFMA32(LDC128(ko + 1024, t * QK_TILE), qf[0][1], s);
#pragma unroll
                for (int r = 0; r < 16; ++r) m = fmaxf(m, s[r]);
            }
            m = fmaxf(m, __shfl_xor(m, 32));
            f32x16 o = zero16;
#pragma nounroll
            for (int t = 0; t < 64; ++t) {
                f32x16 s = MFMA32(LDC128(ko, t * QK_TILE), qf[0][0], zero16);
                s = MFMA32(LDC128(ko + 1024, t * QK_TILE), qf[0][1], s);
                const u32x4 a = LDC128(vo, t * V_TILE), b = LDC128(vo + 1024, t * V_TILE);
#pragma unroll
                for (int r = 0; r < 16; ++r) s[r] = __builtin_amdgcn_exp2f(s[r] - m);
                o = SMFMAC(a, LO8(s), o, ix); o = SMFMAC(b, HI8(s), o, ix);
            }
            l = __shfl(o[12], 32 + l31);
#pragma unroll
            for (int e = 0; e < 4; ++e) { a0[e] = team ? o[8 + e] : o[e]; a1[e] = team ? o[12 + e] : o[4 + e]; }
        }
        const float rl = __builtin_amdgcn_rcpf(l);
        u32x4 v = {pk_bf16(a0[0] * rl, a0[1] * rl), pk_bf16(a0[2] * rl, a0[3] * rl), pk_bf16(a1[0] * rl, a1[1] * rl), pk_bf16(a1[2] * rl, a1[3] * rl)};
#pragma unroll
        for (int e = 0; e < 4; ++e) v[e] = min(v[e], 0xfffffffeu);
        STC128(v, WS_O + (u32)((qt0 + tw) * 32768 + head * 2048 + team * 1024 + lane * 16));
    }
    STAMP(10);
    float g[2], bt[2][4];
    u32x4 a[4];
    const u32 oo = WS_O + (u32)(rt * 32768 + wid * 4096 + lane * 16);
#pragma unroll
    for (int i = 0; i < 4; ++i) a[i] = LDC128(oo + i * 1024, 0);
    SBAR();
#pragma unroll
    for (int r = 0; r < 2; ++r) {
        g[r] = *(LAS float*)(ctl + C_G + (16 * r + (lane & 15)) * 4);
        const float og = rbf(1.f - g[r]);
#pragma unroll
        for (int e = 0; e < 4; ++e) {
            const float xe = (e & 1) ? bf_hi(xv[r][e >> 1]) : bf_lo(xv[r][e >> 1]), de = (e & 1) ? bf_hi(dk[e >> 1]) : bf_lo(dk[e >> 1]);
            bt[r][e] = rbf(og * rbf(bf_lo(yv[r][e]) + rbf(xe * de)));
            PIN(bt[r][e]);
        }
    }
    LGKM0(); __builtin_amdgcn_s_barrier(); asm volatile("" ::: "memory");
#pragma nounroll
    for (u32 sp = 0; sp < SPIN_CAP; ++sp) {
        u32 mx = 0;
#pragma unroll
        for (int i = 0; i < 4; ++i) mx = max(max(mx, max(a[i][0], a[i][1])), max(a[i][2], a[i][3]));
        if (__all(mx != 0xffffffffu)) break;
        TMO(sp, 3);
        asm volatile("" ::: "memory");
#pragma unroll
        for (int i = 0; i < 4; ++i) a[i] = LDC128(oo + i * 1024, 0);
    }
#pragma unroll
    for (int i = 0; i < 4; ++i) *(LAS u32x4*)(sm + wid * 4096 + i * 1024 + lane * 16) = a[i];
    STAMP(11);
    LGKM0(); __builtin_amdgcn_s_barrier(); asm volatile("" ::: "memory");
    if (wid < 7) {
        f32x4 oa[2] = {};
        LAS char* const p = sm + (lane >> 4) * 512 + (lane & 15) * 16;
        u32x4 of[32];
#pragma unroll
        for (int h = 0; h < 32; ++h) of[h] = lds128(p + (h >> 1) * 2048 + (h & 1) * 256);
        SBAR();
#pragma unroll
        for (int h = 0; h < 32; ++h) oa[h & 1] = MFMA16(wof[h >> 1], of[h], oa[h & 1]);
#pragma unroll
        for (int r = 0; r < 2; ++r) {
            float v[4];
#pragma unroll
            for (int e = 0; e < 4; ++e) v[e] = rbf(g[r] * rbf(oa[r][e])) + bt[r][e];
            *(u32x2*)(out + (32 * rt + 16 * r + (lane & 15)) * 896 + c0 * 2) = (u32x2){pk_bf16(v[0], v[1]), pk_bf16(v[2], v[3])};
        }
    }
    asm volatile(".Lcode_end:" ::: "memory");
    STAMP(12);
}

extern "C" void kernel_launch(void* const* d_in, const int* in_sizes, int n_in,
                              void* d_out, int out_size, void* d_ws, size_t ws_size,
                              hipStream_t stream) {
    fused_kernel<<<256, 512, 0, stream>>>((const char*)d_in[0], (const char*)d_in[1], (const char*)d_in[2],
        (const char*)d_in[4], (const char*)d_in[5], (const char*)d_in[6], (const char*)d_in[7], (const char*)d_in[8], (const char*)d_in[9],
        (const char*)d_in[10], (const char*)d_in[11], (const char*)d_in[12], (char*)d_out, (char*)d_ws);
}
```

```cpp
#define DBG_STAMPS
#include <hip/hip_runtime.h>
#include <stdint.h>

typedef unsigned short u16;
typedef unsigned int u32;
typedef unsigned long long u64;
typedef float f32x2 __attribute__((ext_vector_type(2)));
typedef float f32x4 __attribute__((ext_vector_type(4)));
typedef float f32x8 __attribute__((ext_vector_type(8)));
typedef float f32x16 __attribute__((ext_vector_type(16)));
typedef u32 u32x2 __attribute__((ext_vector_type(2)));
typedef u32 u32x4 __attribute__((ext_vector_type(4)));
typedef short s16x4 __attribute__((ext_vector_type(4)));
typedef __bf16 bf16x2 __attribute__((ext_vector_type(2)));
typedef __bf16 bf16x8 __attribute__((ext_vector_type(8)));
typedef __bf16 bf16x16 __attribute__((ext_vector_type(16)));
#define LAS __attribute__((address_space(3)))
#define GAS __attribute__((address_space(1)))
#define DEV __device__ __forceinline__
template <bool B> struct BoolC { static constexpr bool value = B; };
template <int I> struct IntC { static constexpr int value = I; };
template <int I, int N, class F> DEV void static_for(F&& f) { if constexpr (I < N) { f(IntC<I>{}); static_for<I + 1, N>(f); } }

constexpr u32 WS_EPOCH = 0;
constexpr u32 WS_FT = 1024;
constexpr u32 WS_FA = 4096;
constexpr u32 WS_FW = 7168;
constexpr u32 WS_FY = 8192;
constexpr u32 WS_DBG = 16384;
constexpr u32 WS_QK = 1u << 20;
constexpr u32 QK_TILE = 112 * 512;
constexpr u32 WS_V = WS_QK + 64 * QK_TILE;
constexpr u32 V_TILE = 32768;
constexpr u32 WS_O = WS_V + 64 * V_TILE;
constexpr u32 WS_XT = WS_O + 64 * 32768;
constexpr u32 WS_YT = WS_XT + 448 * 4096;
constexpr u32 WS_WOF = WS_YT + 448 * 4096;
constexpr u32 WS_END = WS_WOF + 28 * 16384;

constexpr u32 L_B = 0;
constexpr u32 L_A = 65536;
constexpr u32 L_SLOT = 28672;
constexpr u32 L_TR = L_A + 3 * L_SLOT;
constexpr u32 L_PF = 161792;
constexpr u32 L_CTL = 162816;
constexpr u32 C_A = 0, C_B = 4, C_P0 = 8, C_KV = 16, C_TAGV = 20, C_TAG = 24, C_G = 64;
constexpr u32 B_T = 0, B_KR = 10752, B_BIG = 15360, B_SCR = 8704, B_UC = 4160;
constexpr u32 SPIN_CAP = 1u << 14;

#define MFMA32(a, b, c) __builtin_amdgcn_mfma_f32_32x32x16_bf16(__builtin_bit_cast(bf16x8, a), __builtin_bit_cast(bf16x8, b), c, 0, 0, 0)
#define MFMA16(a, b, c) __builtin_amdgcn_mfma_f32_16x16x32_bf16(__builtin_bit_cast(bf16x8, a), __builtin_bit_cast(bf16x8, b), c, 0, 0, 0)
#define SMFMAC(a, p8, c, ix) __builtin_amdgcn_smfmac_f32_32x32x32_bf16(__builtin_bit_cast(bf16x8, a), __builtin_bit_cast(bf16x16, p8), c, ix, 0, 0)
#define SBAR() __builtin_amdgcn_sched_barrier(0)
#define PIN(x) asm volatile("" : "+v"(x))
#define VMCNT(n) asm volatile("s_waitcnt vmcnt(" #n ")" ::: "memory")
#define LGKM0() asm volatile("s_waitcnt lgkmcnt(0)" ::: "memory")
#define RLX_AGENT __ATOMIC_RELAXED, __HIP_MEMORY_SCOPE_AGENT
#define RLX_WG __ATOMIC_RELAXED, __HIP_MEMORY_SCOPE_WORKGROUP
#define LDC128(off, soff) __builtin_amdgcn_raw_buffer_load_b128(rs, off, soff, 16)
#define STC128(v, off) __builtin_amdgcn_raw_buffer_store_b128(v, rs, off, 0, 16)

DEV u32 pk_bf16(float lo, float hi) { f32x2 v = {lo, hi}; return __builtin_bit_cast(u32, __builtin_convertvector(v, bf16x2)); }
DEV float bf_lo(u32 v) { return __uint_as_float(v << 16); }
DEV float bf_hi(u32 v) { return __uint_as_float(v & 0xffff0000u); }
DEV float rbf(float f) { return bf_lo(pk_bf16(f, f)); }
DEV float dot2(u32 a, u32 b, float c) { return __builtin_amdgcn_fdot2_f32_bf16(__builtin_bit_cast(bf16x2, a), __builtin_bit_cast(bf16x2, b), c, false); }
DEV int rfl(int v) { return __builtin_amdgcn_readfirstlane(v); }
DEV void set_m0(u32 v) { asm volatile("s_mov_b32 m0, %0\n\ts_nop 0" :: "s"(v) : "memory"); }
template <int OFF> DEV void dma16(u32 voff, const void* sbase) {
    asm volatile("global_load_lds_dwordx4 %0, %1 offset:%2" :: "v"(voff), "s"(sbase), "i"(OFF) : "memory"); }
template <int OFF> DEV void put_tid(u32 v) { asm volatile("ds_write_addtid_b32 %0 offset:%1" :: "v"(v), "i"(OFF) : "memory"); }
DEV u32x2 lds_tr(LAS char* p) { return __builtin_bit_cast(u32x2, __builtin_amdgcn_ds_read_tr16_b64_v4i16((LAS s16x4*)p)); }
DEV u32x4 lds128(LAS char* p) { return *(LAS u32x4*)p; }
DEV u32x4 cat(u32x2 a, u32x2 b) { return (u32x4){a[0], a[1], b[0], b[1]}; }
DEV void arrive(LAS char* c) {
    asm volatile("s_mov_b64 exec, 1\n\tds_add_u32 %0, %1\n\ts_mov_b64 exec, -1" :: "v"((u32)(uintptr_t)c), "v"(1u) : "memory"); }
DEV u32 arrive_rtn(LAS char* c) {
    u32 r, s;
    asm volatile("s_mov_b64 exec, 1\n\tds_add_rtn_u32 %0, %2, %3\n\ts_waitcnt lgkmcnt(0)\n\tv_readfirstlane_b32 %1, %0\n\ts_mov_b64 exec, -1\n\ts_nop 3"
                 : "=&v"(r), "=s"(s) : "v"((u32)(uintptr_t)c), "v"(1u) : "memory");
    return s; }
DEV void wait_ge(LAS char* c, u32 target) {
    u32 r, s, n;
    asm volatile("s_mov_b32 %2, 0x400000\n"
                 "1:\n\tds_read_b32 %0, %4\n\ts_sub_u32 %2, %2, 1\n\ts_cselect_b32 %3, 0, %3\n\ts_waitcnt lgkmcnt(0)\n\tv_readfirstlane_b32 %1, %0\n\t"
                 "s_cmp_lt_u32 %1, %3\n\ts_cbranch_scc1 1b"
                 : "=&v"(r), "=&s"(s), "=&s"(n), "+s"(target) : "v"((u32)(uintptr_t)c) : "memory", "scc"); }
DEV u64 get_tag(LAS char* ctl) { wait_ge(ctl + C_TAGV, 1u); return __hip_atomic_load((LAS u64*)(ctl + C_TAG), RLX_WG); }
DEV void put_flag(char* ws, u32 off, u64 tag, int lane) { if (lane == 0) __hip_atomic_store((GAS u64*)(ws + off), tag, RLX_AGENT); }
DEV u64 get_flag(char* ws, u32 off) { return __hip_atomic_load((GAS u64*)(ws + off), RLX_AGENT); }
#define CMUL_PK 0
DEV f32x2 cmul(f32x2 a, f32x2 w) {
    float re, im;
    asm("v_mul_f32 %0, %2, %4\n\tv_mul_f32 %1, %2, %5\n\tv_fma_f32 %0, -%3, %5, %0\n\tv_fmac_f32 %1, %3, %4" : "=&v"(re), "=&v"(im) : "v"(a[0]), "v"(a[1]), "v"(w[0]), "v"(w[1]));
    return (f32x2){re, im};
}
DEV float fmul(float a, float b) { float r; asm("s_nop 0\n\tv_mul_f32 %0, %1, %2" : "=v"(r) : "v"(a), "v"(b)); return r; }
DEV f32x2 zpow(float al, float be, float g) {
    const float m = __builtin_amdgcn_exp2f(al * g), a = __builtin_amdgcn_fractf(be * g);
    return (f32x2){fmul(m, __builtin_amdgcn_cosf(a)), fmul(m, __builtin_amdgcn_sinf(a))}; }

#define STAMP(k) do { u64 a_, b_; asm volatile("s_memrealtime %0\n\ts_memtime %1\n\ts_waitcnt lgkmcnt(0)" : "=s"(a_), "=s"(b_) :: "memory"); stamp[k] = (a_ & 0xffffffffull) | (b_ << 32); } while (0)
#define TMO(sp, k) do {} while (0)

__global__ void __launch_bounds__(512, 2)
fused_kernel(const char* __restrict__ x, const char* __restrict__ wqkv, const char* __restrict__ wo,
             const char* __restrict__ pAre, const char* __restrict__ pAim, const char* __restrict__ pBre, const char* __restrict__ pBim,
             const char* __restrict__ pCre, const char* __restrict__ pCim, const char* __restrict__ logdt, const char* __restrict__ Dskip,
             const char* __restrict__ gatew, char* __restrict__ out, char* ws) {
    __shared__ __attribute__((aligned(256))) char smem_[163840];
    LAS char* const sm = (LAS char*)smem_;
    const u32 sm0 = (u32)(uintptr_t)sm;
    LAS char* const ctl = sm + L_CTL;
    u64 stamp[16]; for (int i = 0; i < 16; ++i) stamp[i] = 0;
    STAMP(0);
    const int lane = threadIdx.x & 63, wid = rfl(threadIdx.x >> 6), team = wid >> 2, tw = wid & 3;
    const int xcd = blockIdx.x & 7, j = blockIdx.x >> 3;
    const int l31 = lane & 31, hh = lane >> 5;
    if (threadIdx.x >= 512 - 6) {
        u64 pc; u32 len;
        asm volatile("s_getpc_b64 %0\n.Lcode_begin:\n\ts_mov_b32 %1, .Lcode_end-.Lcode_begin" : "=s"(pc), "=s"(len));
        set_m0(sm0 + L_PF);
        asm volatile("global_load_lds_dword %0, %1" :: "v"(min((u32)((j + 32 * (63 - lane)) * 128), len)), "s"(pc) : "memory");
    }
    if (threadIdx.x < 16) *(LAS u32*)(ctl + lane * 4) = 0u;
    const bool lo3 = xcd < 3, is3 = xcd == 3, tail7 = xcd == 7 && j >= 18;
    const int jj = tail7 ? j - 18 : j, wd = lo3 ? 5 : is3 ? 8 : tail7 ? 3 : 6, mg = lo3 ? 52 : is3 ? 32 : tail7 ? 86 : 43;
    const int qq = (jj * mg) >> 8;
    const int rb = (lo3 ? 6 * xcd : (is3 || tail7) ? 18 : 5 * xcd - 20) + qq, cb = (xcd < 4 ? 0 : tail7 ? 8 : 5) + jj - qq * wd;
    const bool qv = j < 30 || is3, sv = j < 28;
    const int pw = tw & 1, ch = 56 * xcd + 2 * min(j, 27) + (tw >> 1);
    const int trb = 4 * (xcd < 4 ? 2 * xcd + 1 : 2 * xcd - 8) + (j & 3), tc = min(j >> 2, 6);
    u32 ox[3], ow[4];
    u32 vAre, vAim, vBre, vBim, vCre, vCim, vldt; u64 ep;
    asm volatile("" :: "s"(x), "s"(wqkv), "s"(wo), "s"(pAre), "s"(pAim), "s"(pBre), "s"(pBim), "s"(pCre), "s"(pCim), "s"(logdt), "s"(Dskip), "s"(gatew), "s"(out), "s"(ws));
    if (team) {
        const u32 po = (u32)(ch * 512 + pw * 256 + lane * 4);
        asm volatile("global_load_dword %0, %8, %9\n\tglobal_load_dword %1, %8, %10\n\tglobal_load_ushort %6, %15, %16\n\t"
                     "global_load_dword %2, %8, %11\n\tglobal_load_dword %3, %8, %12\n\tglobal_load_dword %4, %8, %13\n\tglobal_load_dword %5, %8, %14\n\t"
                     "global_load_dwordx2 %7, %17, %18 sc1"
                     : "=&v"(vAre), "=&v"(vAim), "=&v"(vBre), "=&v"(vBim), "=&v"(vCre), "=&v"(vCim), "=&v"(vldt), "=&v"(ep)
                     : "v"(po), "s"(pAre), "s"(pAim), "s"(pBre), "s"(pBim), "s"(pCre), "s"(pCim), "v"((u32)(ch * 2)), "s"(logdt), "v"(0u), "s"(ws) : "memory");
        const u32 o = (u32)((64 * trb + 16 * tw + (lane >> 3)) * 896 + tc * 128 + (lane & 7) * 16);
        set_m0(sm0 + L_TR + tw * 2048);
        dma16<0>(o, x); dma16<1024>(o + 8 * 896 - 1024, x);
    } else {
#pragma unroll
        for (int i = 0; i < 3; ++i) { const int r = 8 * (3 * tw + i) + (lane >> 3); ox[i] = (u32)(min(96 * rb + r, 2047) * 896 + (((lane & 7) ^ ((r >> 1) & 7)) << 4) - i * 1024); }
        ow[0] = (u32)((16 * tw + (lane >> 4)) * 2688 + min(256 * cb + (((lane & 15) ^ ((lane >> 4) << 2)) << 4), 2672));
#pragma unroll
        for (int i = 1; i < 4; ++i) ow[i] = ow[0] + i * (4 * 2688 - 1024);
        if (qv) {
#pragma unroll
            for (int c = 0; c < 2; ++c) {
                const char* xc = x + c * 128; const char* wc = wqkv + c * (64 * 2688);
                set_m0(sm0 + L_A + c * L_SLOT + 12288 + tw * 4096);
                dma16<0>(ow[0], wc); dma16<1024>(ow[1], wc); dma16<2048>(ow[2], wc); dma16<3072>(ow[3], wc);
                set_m0(sm0 + L_A + c * L_SLOT + tw * 3072);
                dma16<0>(ox[0], xc); dma16<1024>(ox[1], xc); dma16<2048>(ox[2], xc);
            }
        }
    }
    LGKM0(); __builtin_amdgcn_s_barrier();
    STAMP(1);
    const __amdgpu_buffer_rsrc_t rs = __builtin_amdgcn_make_buffer_rsrc(ws, 0, (int)WS_END, 0x00020000);
    const int head = 2 * xcd + (j >> 4), qt0 = 4 * (j & 15);
    const int G8 = (7 * head) >> 1;
    const int rt = 8 * xcd + (j >> 2), cq = j & 3;
    u32 cnt = 0;

    if (team == 0) {
        if (qv) {
            const int ct = 4 * cb + tw;
            const bool isV = ct >= 28;
            const int sw = (l31 & 19) | ((l31 & 4) << 1) | ((l31 & 8) >> 1);
            const int xr = isV ? sw : l31, p2 = lane & 3, pp = isV ? p2 : (((p2 & 1) << 1) | (p2 >> 1)), q = (lane & 15) >> 2;
            u32 xa[4];
#pragma unroll
            for (int ks = 0; ks < 4; ++ks) xa[ks] = (u32)(L_A + xr * 128 + (((2 * ks + hh) ^ ((xr >> 1) & 7)) << 4));
            const u32 wa = (u32)(L_A + 12288 + (8 * hh + q) * 256 + ((tw ^ q) << 6) + ((lane >> 4) & 1) * 32 + pp * 8);
            f32x16 acc[3] = {};
            PIN(acc[0]); PIN(acc[1]); PIN(acc[2]);
            auto kloop = [&](auto vo) {
                u32x4 wf[4], xf[4][3];
#define RDW(k, b) wf[k] = cat(lds_tr((b) + wa + (k) * 4096), lds_tr((b) + wa + (k) * 4096 + 1024))
#define RDX(k, i, b) xf[k][i] = lds128((b) + xa[k] + (i) * 4096)
#define MM(k, i) do { SBAR(); if constexpr (decltype(vo)::value) acc[i] = MFMA32(xf[k][i], wf[k], acc[i]); else acc[i] = MFMA32(wf[k], xf[k][i], acc[i]); SBAR(); } while (0)
                VMCNT(7); arrive(ctl + C_A); cnt += 4; wait_ge(ctl + C_A, cnt);
                STAMP(13);
                RDW(0, sm); RDX(0, 0, sm); RDX(0, 1, sm); RDX(0, 2, sm); RDW(1, sm); RDX(1, 0, sm); RDX(1, 1, sm); RDX(1, 2, sm);
                u32 slot = 0, fill = 2 * L_SLOT;
                auto chunk = [&](auto ld, int c) {
                    constexpr bool LD = decltype(ld)::value;
                    LAS char* b = sm + slot;
                    const char* xc = x + (c + 2) * 128; const char* wc = wqkv + (c + 2) * (64 * 2688);
                    MM(0, 0); RDW(2, b); if (LD) set_m0(sm0 + L_A + fill + 12288 + tw * 4096);
                    MM(0, 1); RDX(2, 0, b); RDX(2, 1, b); if (LD) dma16<0>(ow[0], wc);
                    MM(0, 2); RDX(2, 2, b); if (LD) dma16<1024>(ow[1], wc);
                    MM(1, 0); RDW(3, b); if (LD) dma16<2048>(ow[2], wc);
                    MM(1, 1); RDX(3, 0, b); RDX(3, 1, b); if (LD) dma16<3072>(ow[3], wc);
                    MM(1, 2); RDX(3, 2, b); if (LD) VMCNT(4); else VMCNT(0);
                    arrive(ctl + C_A); cnt += 4;
                    MM(2, 0); if (LD) { set_m0(sm0 + L_A + fill + tw * 3072); dma16<0>(ox[0], xc); }
                    MM(2, 1); const u32 cv = *(volatile LAS u32*)(ctl + C_A); if (LD) { dma16<1024>(ox[1], xc); dma16<2048>(ox[2], xc); }
                    MM(2, 2);
                    if (__builtin_expect((u32)rfl((int)cv) < cnt, 0)) wait_ge(ctl + C_A, cnt);
                    fill = slot; slot = slot == 2 * L_SLOT ? 0 : slot + L_SLOT;
                    b = sm + slot;
                    RDW(0, b); RDX(0, 0, b);
                    MM(3, 0); RDX(0, 1, b); RDX(0, 2, b);
                    MM(3, 1); RDW(1, b); RDX(1, 0, b);
                    MM(3, 2); RDX(1, 1, b); RDX(1, 2, b);
                };
#pragma nounroll
                for (int c = 0; c < 5; ++c) chunk(BoolC<true>{}, c);
#pragma nounroll
                for (int c = 5; c < 7; ++c) chunk(BoolC<false>{}, c);
            };
            if (isV) kloop(BoolC<true>{}); else kloop(BoolC<false>{});
            STAMP(3);
            const float sc = ct < 14 ? 0.18898223650461362f * 1.4426950408889634f : 1.0f;
            const int n = (ct - 28) * 32 + l31, nh = (n * 2341) >> 16;
            u32 ob = isV ? WS_V + (u32)(nh * 2048 + hh * 512 + (n - 28 * nh) * 16) : WS_QK + (u32)(ct * 2048 + lane * 16);
            const u32 ts = isV ? V_TILE : QK_TILE;
            ob = ct < 42 ? ob + 3 * rb * ts : 0xf0000000u;
#pragma unroll
            for (int i = 0; i < 3; ++i) {
                const u32 o = (i > 0 && rb == 21) ? 0xf0000000u : ob + i * ts;
#pragma unroll
                for (int s = 0; s < 2; ++s) {
                    u32x4 v;
#pragma unroll
                    for (int e = 0; e < 4; ++e) v[e] = pk_bf16(acc[i][8 * s + 2 * e] * sc, acc[i][8 * s + 2 * e + 1] * sc);
                    STC128(v, o + s * 1024);
                }
            }
            const u64 tag = get_tag(ctl);
            VMCNT(0);
            cnt += 4;
            if (arrive_rtn(ctl + C_A) == cnt - 1) put_flag(ws, WS_FA + (cb * 32 + rb) * 8, tag, lane);
            STAMP(4);
        } else if (j == 31) {
            const int u = xcd < 3 ? xcd : xcd - 1;
            const u32 one = (lane & 3) == 0 ? 0x3f803f80u : 0u;
#pragma nounroll
            for (int i = 4 * u + tw; i < 256; i += 28) STC128(((u32x4){one, one, one, one}), WS_V + (u32)((i * 16 + (lane >> 2)) * 512 + 448 + (lane & 3) * 16));
            VMCNT(0); cnt += 4;
            if (arrive_rtn(ctl + C_A) == cnt - 1) put_flag(ws, WS_FW + (7 + u) * 8, get_tag(ctl), lane);
        }
        {
            const char* xr = x + (32 * rt + 8 * tw + (lane >> 3)) * 896 + (lane & 7) * 16;
            float s = 0.f;
#pragma unroll
            for (int i = 0; i < 7; ++i) {
                const u32x4 a = *(const u32x4*)(xr + i * 128), w = *(const u32x4*)(gatew + (lane & 7) * 16 + i * 128);
#pragma unroll
                for (int e = 0; e < 4; ++e) s = dot2(a[e], w[e], s);
            }
            s += __shfl_xor(s, 1); s += __shfl_xor(s, 2); s += __shfl_xor(s, 4);
            const float g = rbf(__builtin_amdgcn_rcpf(1.f + __builtin_amdgcn_exp2f(-1.4426950408889634f * rbf(s))));
            if ((lane & 7) == 0) *(LAS float*)(ctl + C_G + (8 * tw + (lane >> 3)) * 4) = g;
        }
        STAMP(5);
        if (wid == 0) {
            const u64 tag = get_tag(ctl);
            const int fr = min(l31, 21) * 8;
            const u32 fq = WS_FA + ((8 * G8 + 31 * hh) >> 7) * 256 + fr, fk = WS_FA + ((448 + 8 * G8 + 31 * hh) >> 7) * 256 + fr,
                      fv = WS_FA + ((896 + 28 * head + 27 * hh) >> 7) * 256 + fr, fw = WS_FW + min(lane, 13) * 8;
#pragma nounroll
            for (u32 sp = 0; sp < SPIN_CAP; ++sp) {
                const u64 a = get_flag(ws, fq), b = get_flag(ws, fk), c = get_flag(ws, fv), d = get_flag(ws, fw);
                if (__all(a == tag && b == tag && c == tag && d == tag)) break;
                TMO(sp, 0);
                __builtin_amdgcn_s_sleep(1);
            }
            arrive(ctl + C_KV);
        }
    } else {
        LAS char* const cb_ = sm + L_B + (tw >> 1) * 32768;
        LAS char* const pc = ctl + C_P0 + (tw >> 1) * 4;
        LAS char* const scr = cb_ + B_BIG + pw * B_SCR;
        u32 tix[16];
#pragma unroll
        for (int k = 0; k < 2; ++k)
#pragma unroll
            for (int e = 0; e < 8; e += 2) {
                const int l0 = 2047 - 8 * (128 * pw + 64 * k + lane) - e;
                const u32 b = pk_bf16((float)l0, (float)(l0 - 1));
                tix[8 * k + e] = (u32)max((int)(b & 0xffffu) - 0x4280, min(l0, 128)) * 4u;
                tix[8 * k + e + 1] = (u32)max((int)(b >> 16) - 0x4280, min(l0 - 1, 128)) * 4u;
                PIN(tix[8 * k + e]); PIN(tix[8 * k + e + 1]);
            }
        asm volatile("s_waitcnt vmcnt(0)" : "+v"(vAre), "+v"(vAim), "+v"(vBre), "+v"(vBim), "+v"(vCre), "+v"(vCim), "+v"(vldt), "+v"(ep) :: "memory");
        const u64 tag = ep + 0x9E3779B97F4A7C15ull;
        arrive(ctl + C_B); cnt += 4;
        if (wid == 4) {
            if (lane == 0) __hip_atomic_store((LAS u64*)(ctl + C_TAG), tag, RLX_WG);
            arrive(ctl + C_TAGV);
        }
        {
            const u32x4 ones = {~0u, ~0u, ~0u, ~0u};
            const u32 o = WS_O + (u32)((qt0 + tw) * 32768 + head * 2048 + lane * 16);
            STC128(ones, o); STC128(ones, o + 1024);
        }
        wait_ge(ctl + C_B, cnt);
        if (sv) {
            {
                LAS char* p = sm + L_TR + (8 * (lane >> 4) + ((lane & 15) >> 2)) * 128 + tw * 32 + (lane & 3) * 8;
                const u32 o = WS_XT + (u32)((64 * tc + 16 * tw + (lane & 15)) * 4096 + trb * 128 + (lane >> 4) * 16);
                STC128(cat(lds_tr(p), lds_tr(p + 512)), o);
                STC128(cat(lds_tr(p + 4096), lds_tr(p + 4608)), o + 64);
            }
            STAMP(13);
            const float dt = rbf(expf(bf_lo(vldt)));
            f32x16 kacc = {};
            u32x4 ud[2]; u32x2 up[2];
            const u32 ft = WS_FT + ((ch >> 6) * 32 + l31) * 8;
            set_m0((u32)(uintptr_t)scr);
#pragma nounroll
            for (int r = 0; r < 2; ++r) {
                const u32 sh = r ? 0u : 16u;
                const float al = dt * bf_hi(vAre << sh) * 1.4426950408889634f, be = dt * bf_hi(vAim << sh) * 0.15915494309189535f;
                const f32x2 W = cmul((f32x2){bf_hi(vCre << sh), -bf_hi(vCim << sh)}, (f32x2){fmul(dt, bf_hi(vBre << sh)), fmul(-dt, bf_hi(vBim << sh))});
                const f32x2 z1 = zpow(al, -be, 1.f), z8 = zpow(al, be, 8.f), z256 = zpow(al, -be, 256.f);
#define PUT(row, v) put_tid<(row) * 272>(pk_bf16((v)[0], (v)[1]))
                {
                    f32x2 g0 = W, g1, e0, e4, t;
                    e0 = cmul(g0, z256); g1 = cmul(g0, z1); PUT(0, g0); PUT(8, e0);
                    g0 = cmul(g1, z1); e0 = cmul(e0, z256); PUT(1, g1); PUT(2, g0);
                    t = cmul(g0, z256); g1 = cmul(g0, z1); PUT(12, e0); PUT(9, t);
                    g0 = cmul(g1, z1); e0 = cmul(e0, z256); PUT(3, g1); PUT(4, g0);
                    e4 = cmul(g0, z256); g1 = cmul(g0, z1); PUT(14, e0); PUT(10, e4);
                    g0 = cmul(g1, z1); e0 = cmul(e0, z256); PUT(5, g1); PUT(6, g0);
                    t = cmul(g0, z256); g1 = cmul(g0, z1); PUT(16, e0); PUT(11, t);
                    e4 = cmul(e4, z256); e0 = cmul(e0, z256); PUT(7, g1); PUT(13, e4);
                    e4 = cmul(e4, z256); t = cmul(e0, z256); PUT(17, e0); PUT(15, e4);
                    e0 = cmul(t, z256); PUT(18, t);
                    t = cmul(e0, z256); PUT(19, e0); PUT(20, t);
                }
                u32x4 gf[8];
#pragma unroll
                for (int k = 0; k < 8; ++k) gf[k] = lds128(scr + min(l31, 20) * 272 + hh * 16 + k * 32);
                u64 fl = 0;
                if (r == 0) {
                    VMCNT(0); cnt += 4;
                    if (arrive_rtn(ctl + C_B) == cnt - 1) put_flag(ws, WS_FT + (tc * 32 + trb) * 8, tag, lane);
                } else fl = get_flag(ws, ft);
                {
                    f32x2 f0 = z8, f1;
                    put_tid<0>(0x3f80u);
#define F2(i) f1 = cmul(f0, z8); PUT(i, f0); f0 = cmul(f1, z8); PUT(i + 1, f1);
                    F2(1) F2(3) F2(5) F2(7) F2(9) F2(11) F2(13) F2(15) F2(17) F2(19) F2(21) F2(23) F2(25) F2(27) F2(29)
                    PUT(31, f0);
                }
                u32x4 ff[8];
#pragma unroll
                for (int k = 0; k < 8; ++k) ff[k] = lds128(scr + l31 * 272 + hh * 16 + k * 32);
                if (r == 1) {
#pragma nounroll
                    for (u32 sp = 0; sp < SPIN_CAP; ++sp) { if (__all(fl == tag)) break; TMO(sp, 1); __builtin_amdgcn_s_sleep(1); fl = get_flag(ws, ft); }
                    asm volatile("" ::: "memory");
#pragma unroll
                    for (int k = 0; k < 2; ++k) {
                        const u32 o = WS_XT + (u32)(ch * 4096 + (128 * pw + 64 * k + lane) * 16);
                        ud[k] = LDC128(o, 0);
                        up[k] = __builtin_amdgcn_raw_buffer_load_b64(rs, o - 8, 0, 16);
                    }
                }
#pragma unroll
                for (int k = 0; k < 8; ++k) kacc = MFMA32(gf[k], ff[k], kacc);
            }
            {
                LAS char* const t = cb_ + B_T + pw * 3072;
                *(LAS f32x4*)(t + l31 * 32 + hh * 16) = (f32x4){kacc[0], kacc[1], kacc[2], kacc[3]};
                *(LAS f32x2*)(t + (hh ? 1536 + l31 * 8 : 1024 + l31 * 16)) = (f32x2){kacc[4], kacc[5]};
                *(LAS f32x2*)(t + (hh ? 1792 + l31 * 8 : 1032 + l31 * 16)) = (f32x2){kacc[6], kacc[7]};
#pragma unroll
                for (int a = 0; a < 4; ++a) *(LAS float*)(t + 2048 + hh * 512 + a * 128 + l31 * 4) = kacc[8 + a];
            }
            arrive(pc);
            STAMP(2);
            wait_ge(pc, 2u);
#pragma unroll
            for (int k = 0; k < 2; ++k) {
                u32x4 v;
#pragma unroll
                for (int e = 0; e < 4; ++e) {
                    LAS char* const t0 = cb_ + B_T + tix[8 * k + 2 * e]; LAS char* const t1 = cb_ + B_T + tix[8 * k + 2 * e + 1];
                    v[e] = pk_bf16(*(LAS float*)t0 + *(LAS float*)(t0 + 3072), *(LAS float*)t1 + *(LAS float*)(t1 + 3072));
                }
                *(LAS u32x4*)(cb_ + B_KR + (128 * pw + 64 * k + lane) * 16) = v;
            }
            STAMP(3);
            if (lane < 20) *(LAS u32x4*)(cb_ + (lane < 16 ? B_KR + 4096 + (pw * 16 + lane) * 16 : B_BIG + (lane - 16) * B_UC + pw * 16)) = (u32x4){0u, 0u, 0u, 0u};
            if (pw == 0 && lane == 0) up[0] = (u32x2){0u, 0u};
#pragma unroll
            for (int k = 0; k < 2; ++k) {
                LAS char* const d = cb_ + B_BIG + (128 * pw + 64 * k + lane + 2) * 16;
                const u32 a0 = __builtin_amdgcn_alignbit(up[k][1], up[k][0], 16), a1 = __builtin_amdgcn_alignbit(ud[k][0], up[k][1], 16),
                          a2 = __builtin_amdgcn_alignbit(ud[k][1], ud[k][0], 16), a3 = __builtin_amdgcn_alignbit(ud[k][2], ud[k][1], 16),
                          a4 = __builtin_amdgcn_alignbit(ud[k][3], ud[k][2], 16);
                *(LAS u32x4*)(d) = ud[k];
                *(LAS u32x4*)(d + B_UC) = (u32x4){a1, a2, a3, a4};
                *(LAS u32x4*)(d + 2 * B_UC) = (u32x4){up[k][1], ud[k][0], ud[k][1], ud[k][2]};
                *(LAS u32x4*)(d + 3 * B_UC) = (u32x4){a0, a1, a2, a3};
            }
            arrive(pc); wait_ge(pc, 4u);
            STAMP(4);
            f32x4 ya[8] = {};
            {
                const int p = (lane & 15) + 8 * (lane >> 4) + 1, sg = (-p) & 3;
                LAS char* pa = cb_ + B_BIG + sg * B_UC + (p + sg) * 2 + pw * 256;
                LAS char* pb = cb_ + B_KR + (254 - 2 * (lane & 15) + (lane >> 4) - 224) * 16 + pw * 256;
                u32x4 fa[2][8], fb[2][8];
#define CRD(s, i, d) do { fb[s][i] = lds128(pb + (7 - (i)) * 512 + (d)); fa[s][i] = cat(*(volatile LAS u32x2*)(pa + (i) * 512 + (d)), *(volatile LAS u32x2*)(pa + (i) * 512 + 8 + (d))); } while (0)
#pragma unroll
                for (int i = 0; i < 8; ++i) CRD(0, i, 0);
#pragma nounroll
                for (int mu = 0; mu < 2; ++mu) {
#pragma unroll
                    for (int s = 0; s < 2; ++s) {
#pragma unroll
                        for (int c = 0; c < 8; ++c) {
                            SBAR();
#pragma unroll
                            for (int i = 0; i <= c; ++i) ya[c] = MFMA16(fa[s][i], fb[s][c - i], ya[c]);
                            SBAR();
                            if (c >= 3 && c < 7) { CRD(s ^ 1, 2 * (c - 3), 64 * (s + 1)); CRD(s ^ 1, 2 * (c - 3) + 1, 64 * (s + 1)); }
                        }
                    }
                    pa += 128; pb += 128;
                }
            }
            STAMP(5);
            f32x4 keep[4];
#pragma unroll
            for (int c = 0; c < 4; ++c) {
                *(LAS f32x4*)(cb_ + pw * 4096 + c * 1024 + lane * 16) = pw ? ya[c] : ya[4 + c];
                keep[c] = pw ? ya[4 + c] : ya[c];
            }
            arrive(pc); wait_ge(pc, 6u);
#pragma unroll
            for (int c = 0; c < 4; ++c) {
                const f32x4 v = keep[c] + *(LAS f32x4*)(cb_ + (pw ^ 1) * 4096 + c * 1024 + lane * 16);
                __builtin_amdgcn_raw_buffer_store_b64((u32x2){pk_bf16(v[0], v[1]), pk_bf16(v[2], v[3])}, rs,
                    WS_YT + (u32)(ch * 4096 + (4 * pw + c) * 512 + (lane & 15) * 32 + (lane >> 4) * 8), 0, 16);
            }
            arrive(pc); wait_ge(pc, 8u);
        } else if (j == 31 && xcd != 3) {
            const int u = xcd < 3 ? xcd : xcd - 1;
            const u32 o = (u32)((lane >> 3) * 896 + u * 128 + (lane & 7) * 16);
#pragma nounroll
            for (int i = tw; i < 56; i += 4) { set_m0(sm0 + L_B + i * 1024); dma16<0>(o, wo + i * (8 * 896)); }
            VMCNT(0); arrive(ctl + C_B); cnt += 4; wait_ge(ctl + C_B, cnt);
            const int g = lane >> 4;
            LAS char* p = sm + L_B + (16 * (g >> 1) + 4 * (g & 1) + ((lane & 15) >> 2)) * 128 + tw * 32 + (lane & 3) * 8;
#pragma nounroll
            for (int h = 0; h < 16; ++h) {
                const u32x2 a = lds_tr(p + h * (28 * 128)); u32x2 b = lds_tr(p + h * (28 * 128) + 1024);
                PIN(b);
                if (g == 3) b = (u32x2){0u, 0u};
                STC128(cat(a, b), WS_WOF + (u32)(((4 * u + tw) * 16 + h) * 1024 + lane * 16));
            }
            VMCNT(0); cnt += 4;
            if (arrive_rtn(ctl + C_B) == cnt - 1) put_flag(ws, WS_FW + u * 8, tag, lane);
            wait_ge(ctl + C_B, cnt);
        }
    }
    STAMP(6);

    wait_ge(ctl + C_KV, 1u);
    STAMP(7);
    u32x4 qf[4][2], kf[8][2], vf[8][2], wof[16];
    const u32 qo = WS_QK + (u32)(G8 * 512 + lane * 16), ko = qo + 56 * 512, vo = WS_V + (u32)(head * 2048 + lane * 16);
#define KT(t) (8 * wid + (((t) + j) & 7))
#define LDQ(p) do { const int o_ = (qt0 + ((tw + (p)) & 3)) * QK_TILE; qf[p][0] = LDC128(qo, o_); qf[p][1] = LDC128(qo + 1024, o_); } while (0)
#define LDK(t) do { const int o_ = KT(t) * QK_TILE; kf[t][0] = LDC128(ko, o_); kf[t][1] = LDC128(ko + 1024, o_); } while (0)
#define LDV(t) do { const int o_ = KT(t) * V_TILE; vf[t][0] = LDC128(vo, o_); vf[t][1] = LDC128(vo + 1024, o_); } while (0)
    const u32 m0_ = ((head & 1) && !hh) ? 0u : ~0u, m1_ = (!(head & 1) && hh) ? 0u : ~0u;
#define FIXQ(p) do { qf[p][0][0] &= m0_; qf[p][0][1] &= m0_; qf[p][1][2] &= m1_; qf[p][1][3] &= m1_; } while (0)
    LDQ(0); LDK(0); LDQ(1); LDV(0); LDK(1); LDV(1);
    SBAR();
    u32 ix = 0xddddddddu; PIN(ix);
    f32x16 O[4], S[2];
    const f32x16 zero16 = {};
    O[0] = zero16; O[1] = zero16;
    FIXQ(0);
    S[0] = MFMA32(kf[0][0], qf[0][0], zero16); S[0] = MFMA32(kf[0][1], qf[0][1], S[0]);
    FIXQ(1);
    STAMP(8);
    LAS char* const part = sm + (wid ^ 4) * 16384 + lane * 16;
#define PUTO(p, i) *(LAS f32x4*)(part + ((tw + (p)) & 3) * 4096 + (i) * 1024) = (f32x4){O[p][4 * (i)], O[p][4 * (i) + 1], O[p][4 * (i) + 2], O[p][4 * (i) + 3]}
#define LO8(S) __builtin_shufflevector(S, S, 0, 1, 2, 3, 4, 5, 6, 7)
#define HI8(S) __builtin_shufflevector(S, S, 8, 9, 10, 11, 12, 13, 14, 15)
#define EXP4(S, i) do { SBAR(); S[i] = __builtin_amdgcn_exp2f(S[i]); S[i + 1] = __builtin_amdgcn_exp2f(S[i + 1]); S[i + 2] = __builtin_amdgcn_exp2f(S[i + 2]); S[i + 3] = __builtin_amdgcn_exp2f(S[i + 3]); PIN(S); SBAR(); } while (0)
    u64 fy0 = 0, fy1 = 0, fy2 = 0, fy3 = 0;
    static_for<0, 32>([&](auto ic) {
        constexpr int n = decltype(ic)::value;
        constexpr int P = n < 16 ? (n & 1) : (n >> 3), T = n < 16 ? (n >> 1) : (n & 7);
        constexpr int n1 = n + 1, P1 = n1 < 16 ? (n1 & 1) : (n1 >> 3), T1 = n1 < 16 ? (n1 >> 1) : (n1 & 7);
        constexpr int n0 = n > 0 ? n - 1 : 0, P0 = n0 < 16 ? (n0 & 1) : (n0 >> 3), T0 = n0 < 16 ? (n0 >> 1) : (n0 & 7);
        f32x16& C = S[n & 1]; f32x16& N = S[(n & 1) ^ 1];
        if constexpr (n > 0) { O[P0] = SMFMAC(vf[T0][1], HI8(N), O[P0], ix); PIN(O[P0]); }
        EXP4(C, 0);
        if constexpr (n < 31) { N = MFMA32(kf[T1][0], qf[P1 & 3][0], zero16); PIN(N); }
        if constexpr (n < 12) { if constexpr (n & 1) LDV(T + 2); else LDK(T + 2); }
        if constexpr (n == 12) LDQ(2);
        if constexpr (n == 17) LDQ(3);
        if constexpr (n >= 26 && n < 30) {
#pragma unroll
            for (int h = 0; h < 4; ++h) wof[4 * (n - 26) + h] = LDC128(WS_WOF + (u32)((7 * cq + min(wid, 6)) * 16384 + lane * 16 + h * 1024), (n - 26) * 4096);
        }
        EXP4(C, 4);
        if constexpr (n < 31) { N = MFMA32(kf[T1][1], qf[P1 & 3][1], N); PIN(N); }
        if constexpr (n == 14) FIXQ(2);
        if constexpr (n == 16) { O[2] = zero16; }
        if constexpr (n == 22) FIXQ(3);
        if constexpr (n == 24) { O[3] = zero16; }
        if constexpr (n == 16) if (team) {
            wait_ge(ctl + C_B, cnt); VMCNT(0); cnt += 4;
            if (arrive_rtn(ctl + C_B) == cnt - 1) put_flag(ws, WS_FY + blockIdx.x * 8, get_tag(ctl), lane);
        }
        if constexpr (n == 15) { PUTO(0, 0); PUTO(0, 1); }
        if constexpr (n == 16 || n == 17) { PUTO(1, 2 * (n - 16)); PUTO(1, 2 * (n - 16) + 1); }
        if constexpr (n == 24 || n == 25) { PUTO(2, 2 * (n - 24)); PUTO(2, 2 * (n - 24) + 1); }
        if constexpr (n == 30) if (wid == 7) { fy0 = get_flag(ws, WS_FY + lane * 8); fy1 = get_flag(ws, WS_FY + 512 + lane * 8); fy2 = get_flag(ws, WS_FY + 1024 + lane * 8); fy3 = get_flag(ws, WS_FY + 1536 + lane * 8); }
        EXP4(C, 8);
        O[P] = SMFMAC(vf[T][0], LO8(C), O[P], ix); PIN(O[P]);
        if constexpr (n == 15) { PUTO(0, 2); PUTO(0, 3); }
        EXP4(C, 12);
    });
    O[3] = SMFMAC(vf[7][1], HI8(S[1]), O[3], ix);
    STAMP(9);
#pragma unroll
    for (int i = 0; i < 4; ++i) PUTO(3, i);
    if (wid == 7) {
        const u64 tag = get_tag(ctl);
#pragma nounroll
        for (u32 sp = 0; sp < SPIN_CAP; ++sp) {
            if (__all(fy0 == tag && fy1 == tag && fy2 == tag && fy3 == tag)) break;
            TMO(sp, 2);
            __builtin_amdgcn_s_sleep(1);
            fy0 = get_flag(ws, WS_FY + lane * 8); fy1 = get_flag(ws, WS_FY + 512 + lane * 8); fy2 = get_flag(ws, WS_FY + 1024 + lane * 8); fy3 = get_flag(ws, WS_FY + 1536 + lane * 8);
        }
        if (blockIdx.x == 0) put_flag(ws, WS_EPOCH, tag, lane);
    }
    LGKM0(); __builtin_amdgcn_s_barrier(); asm volatile("" ::: "memory");
    const int c0 = 112 * cq + 16 * min(wid, 6) + 4 * (lane >> 4);
    u32x2 dk, xv[2]; u32 yv[2][4];
    dk = *(const u32x2*)(Dskip + c0 * 2);
#pragma unroll
    for (int r = 0; r < 2; ++r) {
        const int t = 32 * rt + 16 * r + (lane & 15);
        xv[r] = *(const u32x2*)(x + t * 896 + c0 * 2);
#pragma unroll
        for (int e = 0; e < 4; ++e) yv[r][e] = __builtin_amdgcn_raw_buffer_load_b16(rs, WS_YT + (u32)(c0 * 4096 + t * 2), e * 4096, 16);
    }
    SBAR();
    {
        f32x4 a0 = {}, a1 = {}; float l = 0.f;
#pragma unroll
        for (int w = 0; w < 8; ++w) {
            LAS char* const p = sm + w * 16384 + tw * 4096;
            a0 += *(LAS f32x4*)(p + team * 2048 + lane * 16); a1 += *(LAS f32x4*)(p + team * 2048 + 1024 + lane * 16);
            l += *(LAS float*)(p + 3072 + 512 + l31 * 16);
        }
        float mo = fmaxf(fmaxf(fmaxf(fabsf(a0[0]), fabsf(a0[1])), fmaxf(fabsf(a0[2]), fabsf(a0[3]))), fmaxf(fmaxf(fabsf(a1[0]), fabsf(a1[1])), fmaxf(fabsf(a1[2]), fabsf(a1[3]))));
        if (__builtin_expect(__any(!(l < 1.0e18f && l > 1.0e-12f && mo < 3.0e38f)), 0)) {
            const int o_ = (qt0 + tw) * QK_TILE; qf[0][0] = LDC128(qo, o_); qf[0][1] = LDC128(qo + 1024, o_); FIXQ(0);
            float m = -INFINITY;
#pragma nounroll
            for (int t = 0; t < 64; ++t) {
                f32x16 s = MFMA32(LDC128(ko, t * QK_TILE), qf[0][0], zero16);
                s = MFMA32(LDC128(ko + 1024, t * QK_TILE), qf[0][1], s);
#pragma unroll
                for (int r = 0; r < 16; ++r) m = fmaxf(m, s[r]);
            }
            m = fmaxf(m, __shfl_xor(m, 32));
            f32x16 o = zero16;
#pragma nounroll
            for (int t = 0; t < 64; ++t) {
                f32x16 s = MFMA32(LDC128(ko, t * QK_TILE), qf[0][0], zero16);
                s = MFMA32(LDC128(ko + 1024, t * QK_TILE), qf[0][1], s);
                const u32x4 a = LDC128(vo, t * V_TILE), b = LDC128(vo + 1024, t * V_TILE);
#pragma unroll
                for (int r = 0; r < 16; ++r) s[r] = __builtin_amdgcn_exp2f(s[r] - m);
                o = SMFMAC(a, LO8(s), o, ix); o = SMFMAC(b, HI8(s), o, ix);
            }
            l = __shfl(o[12], 32 + l31);
#pragma unroll
            for (int e = 0; e < 4; ++e) { a0[e] = team ? o[8 + e] : o[e]; a1[e] = team ? o[12 + e] : o[4 + e]; }
        }
        const float rl = __builtin_amdgcn_rcpf(l);
        u32x4 v = {pk_bf16(a0[0] * rl, a0[1] * rl), pk_bf16(a0[2] * rl, a0[3] * rl), pk_bf16(a1[0] * rl, a1[1] * rl), pk_bf16(a1[2] * rl, a1[3] * rl)};
#pragma unroll
        for (int e = 0; e < 4; ++e) v[e] = min(v[e], 0xfffffffeu);
        STC128(v, WS_O + (u32)((qt0 + tw) * 32768 + head * 2048 + team * 1024 + lane * 16));
    }
    STAMP(10);
    float g[2], bt[2][4];
    u32x4 a[4];
    const u32 oo = WS_O + (u32)(rt * 32768 + wid * 4096 + lane * 16);
#pragma unroll
    for (int i = 0; i < 4; ++i) a[i] = LDC128(oo + i * 1024, 0);
    SBAR();
#pragma unroll
    for (int r = 0; r < 2; ++r) {
        g[r] = *(LAS float*)(ctl + C_G + (16 * r + (lane & 15)) * 4);
        const float og = rbf(1.f - g[r]);
#pragma unroll
        for (int e = 0; e < 4; ++e) {
            const float xe = (e & 1) ? bf_hi(xv[r][e >> 1]) : bf_lo(xv[r][e >> 1]), de = (e & 1) ? bf_hi(dk[e >> 1]) : bf_lo(dk[e >> 1]);
            bt[r][e] = rbf(og * rbf(bf_lo(yv[r][e]) + rbf(xe * de)));
            PIN(bt[r][e]);
        }
    }
    LGKM0(); __builtin_amdgcn_s_barrier(); asm volatile("" ::: "memory");
#pragma nounroll
    for (u32 sp = 0; sp < SPIN_CAP; ++sp) {
        u32 mx = 0;
#pragma unroll
        for (int i = 0; i < 4; ++i) mx = max(max(mx, max(a[i][0], a[i][1])), max(a[i][2], a[i][3]));
        if (__all(mx != 0xffffffffu)) break;
        TMO(sp, 3);
        asm volatile("" ::: "memory");
#pragma unroll
        for (int i = 0; i < 4; ++i) a[i] = LDC128(oo + i * 1024, 0);
    }
#pragma unroll
    for (int i = 0; i < 4; ++i) *(LAS u32x4*)(sm + wid * 4096 + i * 1024 + lane * 16) = a[i];
    STAMP(11);
    LGKM0(); __builtin_amdgcn_s_barrier(); asm volatile("" ::: "memory");
    if (wid < 7) {
        f32x4 oa[2] = {};
        LAS char* const p = sm + (lane >> 4) * 512 + (lane & 15) * 16;
        u32x4 of[32];
#pragma unroll
        for (int h = 0; h < 32; ++h) of[h] = lds128(p + (h >> 1) * 2048 + (h & 1) * 256);
        SBAR();
#pragma unroll
        for (int h = 0; h < 32; ++h) oa[h & 1] = MFMA16(wof[h >> 1], of[h], oa[h & 1]);
#pragma unroll
        for (int r = 0; r < 2; ++r) {
            float v[4];
#pragma unroll
            for (int e = 0; e < 4; ++e) v[e] = rbf(g[r] * rbf(oa[r][e])) + bt[r][e];
            *(u32x2*)(out + (32 * rt + 16 * r + (lane & 15)) * 896 + c0 * 2) = (u32x2){pk_bf16(v[0], v[1]), pk_bf16(v[2], v[3])};
        }
    }
    asm volatile(".Lcode_end:" ::: "memory");
    STAMP(12);
    if (blockIdx.x == 9 && wid == 0) { const u32 d_ = (u32)stamp[0] - (u32)stamp[0]; const u32 s_ = (u32)__builtin_amdgcn_s_memrealtime(); while ((u32)__builtin_amdgcn_s_memrealtime() - s_ < 8u * min(d_, 10000u)) __builtin_amdgcn_s_sleep(1); }
}

extern "C" void kernel_launch(void* const* d_in, const int* in_sizes, int n_in,
                              void* d_out, int out_size, void* d_ws, size_t ws_size,
                              hipStream_t stream) {
    fused_kernel<<<256, 512, 0, stream>>>((const char*)d_in[0], (const char*)d_in[1], (const char*)d_in[2],
        (const char*)d_in[4], (const char*)d_in[5], (const char*)d_in[6], (const char*)d_in[7], (const char*)d_in[8], (const char*)d_in[9],
        (const char*)d_in[10], (const char*)d_in[11], (const char*)d_in[12], (char*)d_out, (char*)d_ws);
}
```

```cpp
#include <hip/hip_runtime.h>
#include <stdint.h>
#define NO_PF

typedef unsigned short u16;
typedef unsigned int u32;
typedef unsigned long long u64;
typedef float f32x2 __attribute__((ext_vector_type(2)));
typedef float f32x4 __attribute__((ext_vector_type(4)));
typedef float f32x8 __attribute__((ext_vector_type(8)));
typedef float f32x16 __attribute__((ext_vector_type(16)));
typedef u32 u32x2 __attribute__((ext_vector_type(2)));
typedef u32 u32x4 __attribute__((ext_vector_type(4)));
typedef short s16x4 __attribute__((ext_vector_type(4)));
typedef __bf16 bf16x2 __attribute__((ext_vector_type(2)));
typedef __bf16 bf16x8 __attribute__((ext_vector_type(8)));
typedef __bf16 bf16x16 __attribute__((ext_vector_type(16)));
#define LAS __attribute__((address_space(3)))
#define GAS __attribute__((address_space(1)))
#define DEV __device__ __forceinline__
template <bool B> struct BoolC { static constexpr bool value = B; };
template <int I> struct IntC { static constexpr int value = I; };
template <int I, int N, class F> DEV void static_for(F&& f) { if constexpr (I < N) { f(IntC<I>{}); static_for<I + 1, N>(f); } }

constexpr u32 WS_EPOCH = 0;
constexpr u32 WS_FT = 1024;
constexpr u32 WS_FA = 4096;
constexpr u32 WS_FW = 7168;
constexpr u32 WS_FY = 8192;
constexpr u32 WS_DBG = 16384;
constexpr u32 WS_QK = 1u << 20;
constexpr u32 QK_TILE = 112 * 512;
constexpr u32 WS_V = WS_QK + 64 * QK_TILE;
constexpr u32 V_TILE = 32768;
constexpr u32 WS_O = WS_V + 64 * V_TILE;
constexpr u32 WS_XT = WS_O + 64 * 32768;
constexpr u32 WS_YT = WS_XT + 448 * 4096;
constexpr u32 WS_WOF = WS_YT + 448 * 4096;
constexpr u32 WS_END = WS_WOF + 28 * 16384;

constexpr u32 L_B = 0;
constexpr u32 L_A = 65536;
constexpr u32 L_SLOT = 28672;
constexpr u32 L_TR = L_A + 3 * L_SLOT;
constexpr u32 L_PF = 161792;
constexpr u32 L_CTL = 162816;
constexpr u32 C_A = 0, C_B = 4, C_P0 = 8, C_KV = 16, C_TAGV = 20, C_TAG = 24, C_G = 64;
constexpr u32 B_T = 0, B_KR = 10752, B_BIG = 15360, B_SCR = 8704, B_UC = 4160;
constexpr u32 SPIN_CAP = 1u << 14;

#define MFMA32(a, b, c) __builtin_amdgcn_mfma_f32_32x32x16_bf16(__builtin_bit_cast(bf16x8, a), __builtin_bit_cast(bf16x8, b), c, 0, 0, 0)
#define MFMA16(a, b, c) __builtin_amdgcn_mfma_f32_16x16x32_bf16(__builtin_bit_cast(bf16x8, a), __builtin_bit_cast(bf16x8, b), c, 0, 0, 0)
#define SMFMAC(a, p8, c, ix) __builtin_amdgcn_smfmac_f32_32x32x32_bf16(__builtin_bit_cast(bf16x8, a), __builtin_bit_cast(bf16x16, p8), c, ix, 0, 0)
#define SBAR() __builtin_amdgcn_sched_barrier(0)
#define PIN(x) asm volatile("" : "+v"(x))
#define VMCNT(n) asm volatile("s_waitcnt vmcnt(" #n ")" ::: "memory")
#define LGKM0() asm volatile("s_waitcnt lgkmcnt(0)" ::: "memory")
#define RLX_AGENT __ATOMIC_RELAXED, __HIP_MEMORY_SCOPE_AGENT
#define RLX_WG __ATOMIC_RELAXED, __HIP_MEMORY_SCOPE_WORKGROUP
#define LDC128(off, soff) __builtin_amdgcn_raw_buffer_load_b128(rs, off, soff, 16)
#define STC128(v, off) __builtin_amdgcn_raw_buffer_store_b128(v, rs, off, 0, 16)

DEV u32 pk_bf16(float lo, float hi) { f32x2 v = {lo, hi}; return __builtin_bit_cast(u32, __builtin_convertvector(v, bf16x2)); }
DEV float bf_lo(u32 v) { return __uint_as_float(v << 16); }
DEV float bf_hi(u32 v) { return __uint_as_float(v & 0xffff0000u); }
DEV float rbf(float f) { return bf_lo(pk_bf16(f, f)); }
DEV float dot2(u32 a, u32 b, float c) { return __builtin_amdgcn_fdot2_f32_bf16(__builtin_bit_cast(bf16x2, a), __builtin_bit_cast(bf16x2, b), c, false); }
DEV int rfl(int v) { return __builtin_amdgcn_readfirstlane(v); }
DEV void set_m0(u32 v) { asm volatile("s_mov_b32 m0, %0\n\ts_nop 0" :: "s"(v) : "memory"); }
template <int OFF> DEV void dma16(u32 voff, const void* sbase) {
    asm volatile("global_load_lds_dwordx4 %0, %1 offset:%2" :: "v"(voff), "s"(sbase), "i"(OFF) : "memory"); }
template <int OFF> DEV void put_tid(u32 v) { asm volatile("ds_write_addtid_b32 %0 offset:%1" :: "v"(v), "i"(OFF) : "memory"); }
DEV u32x2 lds_tr(LAS char* p) { return __builtin_bit_cast(u32x2, __builtin_amdgcn_ds_read_tr16_b64_v4i16((LAS s16x4*)p)); }
DEV u32x4 lds128(LAS char* p) { return *(LAS u32x4*)p; }
DEV u32x4 cat(u32x2 a, u32x2 b) { return (u32x4){a[0], a[1], b[0], b[1]}; }
DEV void arrive(LAS char* c) {
    asm volatile("s_mov_b64 exec, 1\n\tds_add_u32 %0, %1\n\ts_mov_b64 exec, -1" :: "v"((u32)(uintptr_t)c), "v"(1u) : "memory"); }
DEV u32 arrive_rtn(LAS char* c) {
    u32 r, s;
    asm volatile("s_mov_b64 exec, 1\n\tds_add_rtn_u32 %0, %2, %3\n\ts_waitcnt lgkmcnt(0)\n\tv_readfirstlane_b32 %1, %0\n\ts_mov_b64 exec, -1\n\ts_nop 3"
                 : "=&v"(r), "=s"(s) : "v"((u32)(uintptr_t)c), "v"(1u) : "memory");
    return s; }
DEV void wait_ge(LAS char* c, u32 target) {
    u32 r, s, n;
    asm volatile("s_mov_b32 %2, 0x400000\n"
                 "1:\n\tds_read_b32 %0, %4\n\ts_sub_u32 %2, %2, 1\n\ts_cselect_b32 %3, 0, %3\n\ts_waitcnt lgkmcnt(0)\n\tv_readfirstlane_b32 %1, %0\n\t"
                 "s_cmp_lt_u32 %1, %3\n\ts_cbranch_scc1 1b"
                 : "=&v"(r), "=&s"(s), "=&s"(n), "+s"(target) : "v"((u32)(uintptr_t)c) : "memory", "scc"); }
DEV u64 get_tag(LAS char* ctl) { wait_ge(ctl + C_TAGV, 1u); return __hip_atomic_load((LAS u64*)(ctl + C_TAG), RLX_WG); }
DEV void put_flag(char* ws, u32 off, u64 tag, int lane) { if (lane == 0) __hip_atomic_store((GAS u64*)(ws + off), tag, RLX_AGENT); }
DEV u64 get_flag(char* ws, u32 off) { return __hip_atomic_load((GAS u64*)(ws + off), RLX_AGENT); }
#define CMUL_PK 0
DEV f32x2 cmul(f32x2 a, f32x2 w) {
    float re, im;
    asm("v_mul_f32 %0, %2, %4\n\tv_mul_f32 %1, %2, %5\n\tv_fma_f32 %0, -%3, %5, %0\n\tv_fmac_f32 %1, %3, %4" : "=&v"(re), "=&v"(im) : "v"(a[0]), "v"(a[1]), "v"(w[0]), "v"(w[1]));
    return (f32x2){re, im};
}
DEV float fmul(float a, float b) { float r; asm("s_nop 0\n\tv_mul_f32 %0, %1, %2" : "=v"(r) : "v"(a), "v"(b)); return r; }
DEV f32x2 zpow(float al, float be, float g) {
    const float m = __builtin_amdgcn_exp2f(al * g), a = __builtin_amdgcn_fractf(be * g);
    return (f32x2){fmul(m, __builtin_amdgcn_cosf(a)), fmul(m, __builtin_amdgcn_sinf(a))}; }

#define STAMP(k) do {} while (0)
#define TMO(sp, k) do {} while (0)

__global__ void __launch_bounds__(512, 2)
fused_kernel(const char* __restrict__ x, const char* __restrict__ wqkv, const char* __restrict__ wo,
             const char* __restrict__ pAre, const char* __restrict__ pAim, const char* __restrict__ pBre, const char* __restrict__ pBim,
             const char* __restrict__ pCre, const char* __restrict__ pCim, const char* __restrict__ logdt, const char* __restrict__ Dskip,
             const char* __restrict__ gatew, char* __restrict__ out, char* ws) {
    __shared__ __attribute__((aligned(256))) char smem_[163840];
    LAS char* const sm = (LAS char*)smem_;
    const u32 sm0 = (u32)(uintptr_t)sm;
    LAS char* const ctl = sm + L_CTL;
    STAMP(0);
    const int lane = threadIdx.x & 63, wid = rfl(threadIdx.x >> 6), team = wid >> 2, tw = wid & 3;
    const int xcd = blockIdx.x & 7, j = blockIdx.x >> 3;
    const int l31 = lane & 31, hh = lane >> 5;
    if (threadIdx.x < 16) *(LAS u32*)(ctl + lane * 4) = 0u;
    const bool lo3 = xcd < 3, is3 = xcd == 3, tail7 = xcd == 7 && j >= 18;
    const int jj = tail7 ? j - 18 : j, wd = lo3 ? 5 : is3 ? 8 : tail7 ? 3 : 6, mg = lo3 ? 52 : is3 ? 32 : tail7 ? 86 : 43;
    const int qq = (jj * mg) >> 8;
    const int rb = (lo3 ? 6 * xcd : (is3 || tail7) ? 18 : 5 * xcd - 20) + qq, cb = (xcd < 4 ? 0 : tail7 ? 8 : 5) + jj - qq * wd;
    const bool qv = j < 30 || is3, sv = j < 28;
    const int pw = tw & 1, ch = 56 * xcd + 2 * min(j, 27) + (tw >> 1);
    const int trb = 4 * (xcd < 4 ? 2 * xcd + 1 : 2 * xcd - 8) + (j & 3), tc = min(j >> 2, 6);
    u32 ox[3], ow[4];
    u32 vAre, vAim, vBre, vBim, vCre, vCim, vldt; u64 ep;
    asm volatile("" :: "s"(x), "s"(wqkv), "s"(wo), "s"(pAre), "s"(pAim), "s"(pBre), "s"(pBim), "s"(pCre), "s"(pCim), "s"(logdt), "s"(Dskip), "s"(gatew), "s"(out), "s"(ws));
    if (team) {
        const u32 po = (u32)(ch * 512 + pw * 256 + lane * 4);
        asm volatile("global_load_dword %0, %8, %9\n\tglobal_load_dword %1, %8, %10\n\tglobal_load_ushort %6, %15, %16\n\t"
                     "global_load_dword %2, %8, %11\n\tglobal_load_dword %3, %8, %12\n\tglobal_load_dword %4, %8, %13\n\tglobal_load_dword %5, %8, %14\n\t"
                     "global_load_dwordx2 %7, %17, %18 sc1"
                     : "=&v"(vAre), "=&v"(vAim), "=&v"(vBre), "=&v"(vBim), "=&v"(vCre), "=&v"(vCim), "=&v"(vldt), "=&v"(ep)
                     : "v"(po), "s"(pAre), "s"(pAim), "s"(pBre), "s"(pBim), "s"(pCre), "s"(pCim), "v"((u32)(ch * 2)), "s"(logdt), "v"(0u), "s"(ws) : "memory");
        const u32 o = (u32)((64 * trb + 16 * tw + (lane >> 3)) * 896 + tc * 128 + (lane & 7) * 16);
        set_m0(sm0 + L_TR + tw * 2048);
        dma16<0>(o, x); dma16<1024>(o + 8 * 896 - 1024, x);
    } else {
#pragma unroll
        for (int i = 0; i < 3; ++i) { const int r = 8 * (3 * tw + i) + (lane >> 3); ox[i] = (u32)(min(96 * rb + r, 2047) * 896 + (((lane & 7) ^ ((r >> 1) & 7)) << 4) - i * 1024); }
        ow[0] = (u32)((16 * tw + (lane >> 4)) * 2688 + min(256 * cb + (((lane & 15) ^ ((lane >> 4) << 2)) << 4), 2672));
#pragma unroll
        for (int i = 1; i < 4; ++i) ow[i] = ow[0] + i * (4 * 2688 - 1024);
        if (qv) {
#pragma unroll
            for (int c = 0; c < 2; ++c) {
                const char* xc = x + c * 128; const char* wc = wqkv + c * (64 * 2688);
                set_m0(sm0 + L_A + c * L_SLOT + 12288 + tw * 4096);
                dma16<0>(ow[0], wc); dma16<1024>(ow[1], wc); dma16<2048>(ow[2], wc); dma16<3072>(ow[3], wc);
                set_m0(sm0 + L_A + c * L_SLOT + tw * 3072);
                dma16<0>(ox[0], xc); dma16<1024>(ox[1], xc); dma16<2048>(ox[2], xc);
            }
        }
    }
    LGKM0(); __builtin_amdgcn_s_barrier();
    STAMP(1);
    const __amdgpu_buffer_rsrc_t rs = __builtin_amdgcn_make_buffer_rsrc(ws, 0, (int)WS_END, 0x00020000);
    const int head = 2 * xcd + (j >> 4), qt0 = 4 * (j & 15);
    const int G8 = (7 * head) >> 1;
    const int rt = 8 * xcd + (j >> 2), cq = j & 3;
    u32 cnt = 0;

    if (team == 0) {
        if (qv) {
            const int ct = 4 * cb + tw;
            const bool isV = ct >= 28;
            const int sw = (l31 & 19) | ((l31 & 4) << 1) | ((l31 & 8) >> 1);
            const int xr = isV ? sw : l31, p2 = lane & 3, pp = isV ? p2 : (((p2 & 1) << 1) | (p2 >> 1)), q = (lane & 15) >> 2;
            u32 xa[4];
#pragma unroll
            for (int ks = 0; ks < 4; ++ks) xa[ks] = (u32)(L_A + xr * 128 + (((2 * ks + hh) ^ ((xr >> 1) & 7)) << 4));
            const u32 wa = (u32)(L_A + 12288 + (8 * hh + q) * 256 + ((tw ^ q) << 6) + ((lane >> 4) & 1) * 32 + pp * 8);
            f32x16 acc[3] = {};
            PIN(acc[0]); PIN(acc[1]); PIN(acc[2]);
            auto kloop = [&](auto vo) {
                u32x4 wf[4], xf[4][3];
#define RDW(k, b) wf[k] = cat(lds_tr((b) + wa + (k) * 4096), lds_tr((b) + wa + (k) * 4096 + 1024))
#define RDX(k, i, b) xf[k][i] = lds128((b) + xa[k] + (i) * 4096)
#define MM(k, i) do { SBAR(); if constexpr (decltype(vo)::value) acc[i] = MFMA32(xf[k][i], wf[k], acc[i]); else acc[i] = MFMA32(wf[k], xf[k][i], acc[i]); SBAR(); } while (0)
                VMCNT(7); arrive(ctl + C_A); cnt += 4; wait_ge(ctl + C_A, cnt);
                STAMP(13);
                RDW(0, sm); RDX(0, 0, sm); RDX(0, 1, sm); RDX(0, 2, sm); RDW(1, sm); RDX(1, 0, sm); RDX(1, 1, sm); RDX(1, 2, sm);
                u32 slot = 0, fill = 2 * L_SLOT;
                auto chunk = [&](auto ld, int c) {
                    constexpr bool LD = decltype(ld)::value;
                    LAS char* b = sm + slot;
                    const char* xc = x + (c + 2) * 128; const char* wc = wqkv + (c + 2) * (64 * 2688);
                    MM(0, 0); RDW(2, b); if (LD) set_m0(sm0 + L_A + fill + 12288 + tw * 4096);
                    MM(0, 1); RDX(2, 0, b); RDX(2, 1, b); if (LD) dma16<0>(ow[0], wc);
                    MM(0, 2); RDX(2, 2, b); if (LD) dma16<1024>(ow[1], wc);
                    MM(1, 0); RDW(3, b); if (LD) dma16<2048>(ow[2], wc);
                    MM(1, 1); RDX(3, 0, b); RDX(3, 1, b); if (LD) dma16<3072>(ow[3], wc);
                    MM(1, 2); RDX(3, 2, b); if (LD) VMCNT(4); else VMCNT(0);
                    arrive(ctl + C_A); cnt += 4;
                    MM(2, 0); if (LD) { set_m0(sm0 + L_A + fill + tw * 3072); dma16<0>(ox[0], xc); }
                    MM(2, 1); const u32 cv = *(volatile LAS u32*)(ctl + C_A); if (LD) { dma16<1024>(ox[1], xc); dma16<2048>(ox[2], xc); }
                    MM(2, 2);
                    if (__builtin_expect((u32)rfl((int)cv) < cnt, 0)) wait_ge(ctl + C_A, cnt);
                    fill = slot; slot = slot == 2 * L_SLOT ? 0 : slot + L_SLOT;
                    b = sm + slot;
                    RDW(0, b); RDX(0, 0, b);
                    MM(3, 0); RDX(0, 1, b); RDX(0, 2, b);
                    MM(3, 1); RDW(1, b); RDX(1, 0, b);
                    MM(3, 2); RDX(1, 1, b); RDX(1, 2, b);
                };
#pragma nounroll
                for (int c = 0; c < 5; ++c) chunk(BoolC<true>{}, c);
#pragma nounroll
                for (int c = 5; c < 7; ++c) chunk(BoolC<false>{}, c);
            };
            if (isV) kloop(BoolC<true>{}); else kloop(BoolC<false>{});
            STAMP(3);
            const float sc = ct < 14 ? 0.18898223650461362f * 1.4426950408889634f : 1.0f;
            const int n = (ct - 28) * 32 + l31, nh = (n * 2341) >> 16;
            u32 ob = isV ? WS_V + (u32)(nh * 2048 + hh * 512 + (n - 28 * nh) * 16) : WS_QK + (u32)(ct * 2048 + lane * 16);
            const u32 ts = isV ? V_TILE : QK_TILE;
            ob = ct < 42 ? ob + 3 * rb * ts : 0xf0000000u;
#pragma unroll
            for (int i = 0; i < 3; ++i) {
                const u32 o = (i > 0 && rb == 21) ? 0xf0000000u : ob + i * ts;
#pragma unroll
                for (int s = 0; s < 2; ++s) {
                    u32x4 v;
#pragma unroll
                    for (int e = 0; e < 4; ++e) v[e] = pk_bf16(acc[i][8 * s + 2 * e] * sc, acc[i][8 * s + 2 * e + 1] * sc);
                    STC128(v, o + s * 1024);
                }
            }
            const u64 tag = get_tag(ctl);
            VMCNT(0);
            cnt += 4;
            if (arrive_rtn(ctl + C_A) == cnt - 1) put_flag(ws, WS_FA + (cb * 32 + rb) * 8, tag, lane);
            STAMP(4);
        } else if (j == 31) {
            const int u = xcd < 3 ? xcd : xcd - 1;
            const u32 one = (lane & 3) == 0 ? 0x3f803f80u : 0u;
#pragma nounroll
            for (int i = 4 * u + tw; i < 256; i += 28) STC128(((u32x4){one, one, one, one}), WS_V + (u32)((i * 16 + (lane >> 2)) * 512 + 448 + (lane & 3) * 16));
            VMCNT(0); cnt += 4;
            if (arrive_rtn(ctl + C_A) == cnt - 1) put_flag(ws, WS_FW + (7 + u) * 8, get_tag(ctl), lane);
        }
        {
            const char* xr = x + (32 * rt + 8 * tw + (lane >> 3)) * 896 + (lane & 7) * 16;
            float s = 0.f;
#pragma unroll
            for (int i = 0; i < 7; ++i) {
                const u32x4 a = *(const u32x4*)(xr + i * 128), w = *(const u32x4*)(gatew + (lane & 7) * 16 + i * 128);
#pragma unroll
                for (int e = 0; e < 4; ++e) s = dot2(a[e], w[e], s);
            }
            s += __shfl_xor(s, 1); s += __shfl_xor(s, 2); s += __shfl_xor(s, 4);
            const float g = rbf(__builtin_amdgcn_rcpf(1.f + __builtin_amdgcn_exp2f(-1.4426950408889634f * rbf(s))));
            if ((lane & 7) == 0) *(LAS float*)(ctl + C_G + (8 * tw + (lane >> 3)) * 4) = g;
        }
        STAMP(5);
        if (wid == 0) {
            const u64 tag = get_tag(ctl);
            const int fr = min(l31, 21) * 8;
            const u32 fq = WS_FA + ((8 * G8 + 31 * hh) >> 7) * 256 + fr, fk = WS_FA + ((448 + 8 * G8 + 31 * hh) >> 7) * 256 + fr,
                      fv = WS_FA + ((896 + 28 * head + 27 * hh) >> 7) * 256 + fr, fw = WS_FW + min(lane, 13) * 8;
#pragma nounroll
            for (u32 sp = 0; sp < SPIN_CAP; ++sp) {
                const u64 a = get_flag(ws, fq), b = get_flag(ws, fk), c = get_flag(ws, fv), d = get_flag(ws, fw);
                if (__all(a == tag && b == tag && c == tag && d == tag)) break;
                TMO(sp, 0);
                __builtin_amdgcn_s_sleep(1);
            }
            arrive(ctl + C_KV);
        }
    } else {
        LAS char* const cb_ = sm + L_B + (tw >> 1) * 32768;
        LAS char* const pc = ctl + C_P0 + (tw >> 1) * 4;
        LAS char* const scr = cb_ + B_BIG + pw * B_SCR;
        u32 tix[16];
#pragma unroll
        for (int k = 0; k < 2; ++k)
#pragma unroll
            for (int e = 0; e < 8; e += 2) {
                const int l0 = 2047 - 8 * (128 * pw + 64 * k + lane) - e;
                const u32 b = pk_bf16((float)l0, (float)(l0 - 1));
                tix[8 * k + e] = (u32)max((int)(b & 0xffffu) - 0x4280, min(l0, 128)) * 4u;
                tix[8 * k + e + 1] = (u32)max((int)(b >> 16) - 0x4280, min(l0 - 1, 128)) * 4u;
                PIN(tix[8 * k + e]); PIN(tix[8 * k + e + 1]);
            }
        asm volatile("s_waitcnt vmcnt(0)" : "+v"(vAre), "+v"(vAim), "+v"(vBre), "+v"(vBim), "+v"(vCre), "+v"(vCim), "+v"(vldt), "+v"(ep) :: "memory");
        const u64 tag = ep + 0x9E3779B97F4A7C15ull;
        arrive(ctl + C_B); cnt += 4;
        if (wid == 4) {
            if (lane == 0) __hip_atomic_store((LAS u64*)(ctl + C_TAG), tag, RLX_WG);
            arrive(ctl + C_TAGV);
        }
        {
            const u32x4 ones = {~0u, ~0u, ~0u, ~0u};
            const u32 o = WS_O + (u32)((qt0 + tw) * 32768 + head * 2048 + lane * 16);
            STC128(ones, o); STC128(ones, o + 1024);
        }
        wait_ge(ctl + C_B, cnt);
        if (sv) {
            {
                LAS char* p = sm + L_TR + (8 * (lane >> 4) + ((lane & 15) >> 2)) * 128 + tw * 32 + (lane & 3) * 8;
                const u32 o = WS_XT + (u32)((64 * tc + 16 * tw + (lane & 15)) * 4096 + trb * 128 + (lane >> 4) * 16);
                STC128(cat(lds_tr(p), lds_tr(p + 512)), o);
                STC128(cat(lds_tr(p + 4096), lds_tr(p + 4608)), o + 64);
            }
            STAMP(13);
            const float dt = rbf(expf(bf_lo(vldt)));
            f32x16 kacc = {};
            u32x4 ud[2]; u32x2 up[2];
            const u32 ft = WS_FT + ((ch >> 6) * 32 + l31) * 8;
            set_m0((u32)(uintptr_t)scr);
#pragma nounroll
            for (int r = 0; r < 2; ++r) {
                const u32 sh = r ? 0u : 16u;
                const float al = dt * bf_hi(vAre << sh) * 1.4426950408889634f, be = dt * bf_hi(vAim << sh) * 0.15915494309189535f;
                const f32x2 W = cmul((f32x2){bf_hi(vCre << sh), -bf_hi(vCim << sh)}, (f32x2){fmul(dt, bf_hi(vBre << sh)), fmul(-dt, bf_hi(vBim << sh))});
                const f32x2 z1 = zpow(al, -be, 1.f), z8 = zpow(al, be, 8.f), z256 = zpow(al, -be, 256.f);
#define PUT(row, v) put_tid<(row) * 272>(pk_bf16((v)[0], (v)[1]))
                {
                    f32x2 g0 = W, g1, e0, e4, t;
                    e0 = cmul(g0, z256); g1 = cmul(g0, z1); PUT(0, g0); PUT(8, e0);
                    g0 = cmul(g1, z1); e0 = cmul(e0, z256); PUT(1, g1); PUT(2, g0);
                    t = cmul(g0, z256); g1 = cmul(g0, z1); PUT(12, e0); PUT(9, t);
                    g0 = cmul(g1, z1); e0 = cmul(e0, z256); PUT(3, g1); PUT(4, g0);
                    e4 = cmul(g0, z256); g1 = cmul(g0, z1); PUT(14, e0); PUT(10, e4);
                    g0 = cmul(g1, z1); e0 = cmul(e0, z256); PUT(5, g1); PUT(6, g0);
                    t = cmul(g0, z256); g1 = cmul(g0, z1); PUT(16, e0); PUT(11, t);
                    e4 = cmul(e4, z256); e0 = cmul(e0, z256); PUT(7, g1); PUT(13, e4);
                    e4 = cmul(e4, z256); t = cmul(e0, z256); PUT(17, e0); PUT(15, e4);
                    e0 = cmul(t, z256); PUT(18, t);
                    t = cmul(e0, z256); PUT(19, e0); PUT(20, t);
                }
                u32x4 gf[8];
#pragma unroll
                for (int k = 0; k < 8; ++k) gf[k] = lds128(scr + min(l31, 20) * 272 + hh * 16 + k * 32);
                u64 fl = 0;
                if (r == 0) {
                    VMCNT(0); cnt += 4;
                    if (arrive_rtn(ctl + C_B) == cnt - 1) put_flag(ws, WS_FT + (tc * 32 + trb) * 8, tag, lane);
                } else fl = get_flag(ws, ft);
                {
                    f32x2 f0 = z8, f1;
                    put_tid<0>(0x3f80u);
#define F2(i) f1 = cmul(f0, z8); PUT(i, f0); f0 = cmul(f1, z8); PUT(i + 1, f1);
                    F2(1) F2(3) F2(5) F2(7) F2(9) F2(11) F2(13) F2(15) F2(17) F2(19) F2(21) F2(23) F2(25) F2(27) F2(29)
                    PUT(31, f0);
                }
                u32x4 ff[8];
#pragma unroll
                for (int k = 0; k < 8; ++k) ff[k] = lds128(scr + l31 * 272 + hh * 16 + k * 32);
                if (r == 1) {
#pragma nounroll
                    for (u32 sp = 0; sp < SPIN_CAP; ++sp) { if (__all(fl == tag)) break; TMO(sp, 1); __builtin_amdgcn_s_sleep(1); fl = get_flag(ws, ft); }
                    asm volatile("" ::: "memory");
#pragma unroll
                    for (int k = 0; k < 2; ++k) {
                        const u32 o = WS_XT + (u32)(ch * 4096 + (128 * pw + 64 * k + lane) * 16);
                        ud[k] = LDC128(o, 0);
                        up[k] = __builtin_amdgcn_raw_buffer_load_b64(rs, o - 8, 0, 16);
                    }
                }
#pragma unroll
                for (int k = 0; k < 8; ++k) kacc = MFMA32(gf[k], ff[k], kacc);
            }
            {
                LAS char* const t = cb_ + B_T + pw * 3072;
                *(LAS f32x4*)(t + l31 * 32 + hh * 16) = (f32x4){kacc[0], kacc[1], kacc[2], kacc[3]};
                *(LAS f32x2*)(t + (hh ? 1536 + l31 * 8 : 1024 + l31 * 16)) = (f32x2){kacc[4], kacc[5]};
                *(LAS f32x2*)(t + (hh ? 1792 + l31 * 8 : 1032 + l31 * 16)) = (f32x2){kacc[6], kacc[7]};
#pragma unroll
                for (int a = 0; a < 4; ++a) *(LAS float*)(t + 2048 + hh * 512 + a * 128 + l31 * 4) = kacc[8 + a];
            }
            arrive(pc);
            STAMP(2);
            wait_ge(pc, 2u);
#pragma unroll
            for (int k = 0; k < 2; ++k) {
                u32x4 v;
#pragma unroll
                for (int e = 0; e < 4; ++e) {
                    LAS char* const t0 = cb_ + B_T + tix[8 * k + 2 * e]; LAS char* const t1 = cb_ + B_T + tix[8 * k + 2 * e + 1];
                    v[e] = pk_bf16(*(LAS float*)t0 + *(LAS float*)(t0 + 3072), *(LAS float*)t1 + *(LAS float*)(t1 + 3072));
                }
                *(LAS u32x4*)(cb_ + B_KR + (128 * pw + 64 * k + lane) * 16) = v;
            }
            STAMP(3);
            if (lane < 20) *(LAS u32x4*)(cb_ + (lane < 16 ? B_KR + 4096 + (pw * 16 + lane) * 16 : B_BIG + (lane - 16) * B_UC + pw * 16)) = (u32x4){0u, 0u, 0u, 0u};
            if (pw == 0 && lane == 0) up[0] = (u32x2){0u, 0u};
#pragma unroll
            for (int k = 0; k < 2; ++k) {
                LAS char* const d = cb_ + B_BIG + (128 * pw + 64 * k + lane + 2) * 16;
                const u32 a0 = __builtin_amdgcn_alignbit(up[k][1], up[k][0], 16), a1 = __builtin_amdgcn_alignbit(ud[k][0], up[k][1], 16),
                          a2 = __builtin_amdgcn_alignbit(ud[k][1], ud[k][0], 16), a3 = __builtin_amdgcn_alignbit(ud[k][2], ud[k][1], 16),
                          a4 = __builtin_amdgcn_alignbit(ud[k][3], ud[k][2], 16);
                *(LAS u32x4*)(d) = ud[k];
                *(LAS u32x4*)(d + B_UC) = (u32x4){a1, a2, a3, a4};
                *(LAS u32x4*)(d + 2 * B_UC) = (u32x4){up[k][1], ud[k][0], ud[k][1], ud[k][2]};
                *(LAS u32x4*)(d + 3 * B_UC) = (u32x4){a0, a1, a2, a3};
            }
            arrive(pc); wait_ge(pc, 4u);
            STAMP(4);
            f32x4 ya[8] = {};
            {
                const int p = (lane & 15) + 8 * (lane >> 4) + 1, sg = (-p) & 3;
                LAS char* pa = cb_ + B_BIG + sg * B_UC + (p + sg) * 2 + pw * 256;
                LAS char* pb = cb_ + B_KR + (254 - 2 * (lane & 15) + (lane >> 4) - 224) * 16 + pw * 256;
                u32x4 fa[2][8], fb[2][8];
#define CRD(s, i, d) do { fb[s][i] = lds128(pb + (7 - (i)) * 512 + (d)); fa[s][i] = cat(*(volatile LAS u32x2*)(pa + (i) * 512 + (d)), *(volatile LAS u32x2*)(pa + (i) * 512 + 8 + (d))); } while (0)
#pragma unroll
                for (int i = 0; i < 8; ++i) CRD(0, i, 0);
#pragma nounroll
                for (int mu = 0; mu < 2; ++mu) {
#pragma unroll
                    for (int s = 0; s < 2; ++s) {
#pragma unroll
                        for (int c = 0; c < 8; ++c) {
                            SBAR();
#pragma unroll
                            for (int i = 0; i <= c; ++i) ya[c] = MFMA16(fa[s][i], fb[s][c - i], ya[c]);
                            SBAR();
                            if (c >= 3 && c < 7) { CRD(s ^ 1, 2 * (c - 3), 64 * (s + 1)); CRD(s ^ 1, 2 * (c - 3) + 1, 64 * (s + 1)); }
                        }
                    }
                    pa += 128; pb += 128;
                }
            }
            STAMP(5);
            f32x4 keep[4];
#pragma unroll
            for (int c = 0; c < 4; ++c) {
                *(LAS f32x4*)(cb_ + pw * 4096 + c * 1024 + lane * 16) = pw ? ya[c] : ya[4 + c];
                keep[c] = pw ? ya[4 + c] : ya[c];
            }
            arrive(pc); wait_ge(pc, 6u);
#pragma unroll
            for (int c = 0; c < 4; ++c) {
                const f32x4 v = keep[c] + *(LAS f32x4*)(cb_ + (pw ^ 1) * 4096 + c * 1024 + lane * 16);
                __builtin_amdgcn_raw_buffer_store_b64((u32x2){pk_bf16(v[0], v[1]), pk_bf16(v[2], v[3])}, rs,
                    WS_YT + (u32)(ch * 4096 + (4 * pw + c) * 512 + (lane & 15) * 32 + (lane >> 4) * 8), 0, 16);
            }
            arrive(pc); wait_ge(pc, 8u);
        } else if (j == 31 && xcd != 3) {
            const int u = xcd < 3 ? xcd : xcd - 1;
            const u32 o = (u32)((lane >> 3) * 896 + u * 128 + (lane & 7) * 16);
#pragma nounroll
            for (int i = tw; i < 56; i += 4) { set_m0(sm0 + L_B + i * 1024); dma16<0>(o, wo + i * (8 * 896)); }
            VMCNT(0); arrive(ctl + C_B); cnt += 4; wait_ge(ctl + C_B, cnt);
            const int g = lane >> 4;
            LAS char* p = sm + L_B + (16 * (g >> 1) + 4 * (g & 1) + ((lane & 15) >> 2)) * 128 + tw * 32 + (lane & 3) * 8;
#pragma nounroll
            for (int h = 0; h < 16; ++h) {
                const u32x2 a = lds_tr(p + h * (28 * 128)); u32x2 b = lds_tr(p + h * (28 * 128) + 1024);
                PIN(b);
                if (g == 3) b = (u32x2){0u, 0u};
                STC128(cat(a, b), WS_WOF + (u32)(((4 * u + tw) * 16 + h) * 1024 + lane * 16));
            }
            VMCNT(0); cnt += 4;
            if (arrive_rtn(ctl + C_B) == cnt - 1) put_flag(ws, WS_FW + u * 8, tag, lane);
            wait_ge(ctl + C_B, cnt);
        }
    }
    STAMP(6);

    wait_ge(ctl + C_KV, 1u);
    STAMP(7);
    u32x4 qf[4][2], kf[8][2], vf[8][2], wof[16];
    const u32 qo = WS_QK + (u32)(G8 * 512 + lane * 16), ko = qo + 56 * 512, vo = WS_V + (u32)(head * 2048 + lane * 16);
#define KT(t) (8 * wid + (((t) + j) & 7))
#define LDQ(p) do { const int o_ = (qt0 + ((tw + (p)) & 3)) * QK_TILE; qf[p][0] = LDC128(qo, o_); qf[p][1] = LDC128(qo + 1024, o_); } while (0)
#define LDK(t) do { const int o_ = KT(t) * QK_TILE; kf[t][0] = LDC128(ko, o_); kf[t][1] = LDC128(ko + 1024, o_); } while (0)
#define LDV(t) do { const int o_ = KT(t) * V_TILE; vf[t][0] = LDC128(vo, o_); vf[t][1] = LDC128(vo + 1024, o_); } while (0)
    const u32 m0_ = ((head & 1) && !hh) ? 0u : ~0u, m1_ = (!(head & 1) && hh) ? 0u : ~0u;
#define FIXQ(p) do { qf[p][0][0] &= m0_; qf[p][0][1] &= m0_; qf[p][1][2] &= m1_; qf[p][1][3] &= m1_; } while (0)
    LDQ(0); LDK(0); LDQ(1); LDV(0); LDK(1); LDV(1);
    SBAR();
    u32 ix = 0xddddddddu; PIN(ix);
    f32x16 O[4], S[2];
    const f32x16 zero16 = {};
    O[0] = zero16; O[1] = zero16;
    FIXQ(0);
    S[0] = MFMA32(kf[0][0], qf[0][0], zero16); S[0] = MFMA32(kf[0][1], qf[0][1], S[0]);
    FIXQ(1);
    STAMP(8);
    LAS char* const part = sm + (wid ^ 4) * 16384 + lane * 16;
#define PUTO(p, i) *(LAS f32x4*)(part + ((tw + (p)) & 3) * 4096 + (i) * 1024) = (f32x4){O[p][4 * (i)], O[p][4 * (i) + 1], O[p][4 * (i) + 2], O[p][4 * (i) + 3]}
#define LO8(S) __builtin_shufflevector(S, S, 0, 1, 2, 3, 4, 5, 6, 7)
#define HI8(S) __builtin_shufflevector(S, S, 8, 9, 10, 11, 12, 13, 14, 15)
#define EXP4(S, i) do { SBAR(); S[i] = __builtin_amdgcn_exp2f(S[i]); S[i + 1] = __builtin_amdgcn_exp2f(S[i + 1]); S[i + 2] = __builtin_amdgcn_exp2f(S[i + 2]); S[i + 3] = __builtin_amdgcn_exp2f(S[i + 3]); PIN(S); SBAR(); } while (0)
    u64 fy0 = 0, fy1 = 0, fy2 = 0, fy3 = 0;
    static_for<0, 32>([&](auto ic) {
        constexpr int n = decltype(ic)::value;
        constexpr int P = n < 16 ? (n & 1) : (n >> 3), T = n < 16 ? (n >> 1) : (n & 7);
        constexpr int n1 = n + 1, P1 = n1 < 16 ? (n1 & 1) : (n1 >> 3), T1 = n1 < 16 ? (n1 >> 1) : (n1 & 7);
        constexpr int n0 = n > 0 ? n - 1 : 0, P0 = n0 < 16 ? (n0 & 1) : (n0 >> 3), T0 = n0 < 16 ? (n0 >> 1) : (n0 & 7);
        f32x16& C = S[n & 1]; f32x16& N = S[(n & 1) ^ 1];
        if constexpr (n > 0) { O[P0] = SMFMAC(vf[T0][1], HI8(N), O[P0], ix); PIN(O[P0]); }
        EXP4(C, 0);
        if constexpr (n < 31) { N = MFMA32(kf[T1][0], qf[P1 & 3][0], zero16); PIN(N); }
        if constexpr (n < 12) { if constexpr (n & 1) LDV(T + 2); else LDK(T + 2); }
        if constexpr (n == 12) LDQ(2);
        if constexpr (n == 17) LDQ(3);
        if constexpr (n >= 26 && n < 30) {
#pragma unroll
            for (int h = 0; h < 4; ++h) wof[4 * (n - 26) + h] = LDC128(WS_WOF + (u32)((7 * cq + min(wid, 6)) * 16384 + lane * 16 + h * 1024), (n - 26) * 4096);
        }
        EXP4(C, 4);
        if constexpr (n < 31) { N = MFMA32(kf[T1][1], qf[P1 & 3][1], N); PIN(N); }
        if constexpr (n == 14) FIXQ(2);
        if constexpr (n == 16) { O[2] = zero16; }
        if constexpr (n == 22) FIXQ(3);
        if constexpr (n == 24) { O[3] = zero16; }
        if constexpr (n == 16) if (team) {
            wait_ge(ctl + C_B, cnt); VMCNT(0); cnt += 4;
            if (arrive_rtn(ctl + C_B) == cnt - 1) put_flag(ws, WS_FY + blockIdx.x * 8, get_tag(ctl), lane);
        }
        if constexpr (n == 15) { PUTO(0, 0); PUTO(0, 1); }
        if constexpr (n == 16 || n == 17) { PUTO(1, 2 * (n - 16)); PUTO(1, 2 * (n - 16) + 1); }
        if constexpr (n == 24 || n == 25) { PUTO(2, 2 * (n - 24)); PUTO(2, 2 * (n - 24) + 1); }
        if constexpr (n == 30) if (wid == 7) { fy0 = get_flag(ws, WS_FY + lane * 8); fy1 = get_flag(ws, WS_FY + 512 + lane * 8); fy2 = get_flag(ws, WS_FY + 1024 + lane * 8); fy3 = get_flag(ws, WS_FY + 1536 + lane * 8); }
        EXP4(C, 8);
        O[P] = SMFMAC(vf[T][0], LO8(C), O[P], ix); PIN(O[P]);
        if constexpr (n == 15) { PUTO(0, 2); PUTO(0, 3); }
        EXP4(C, 12);
    });
    O[3] = SMFMAC(vf[7][1], HI8(S[1]), O[3], ix);
    STAMP(9);
#pragma unroll
    for (int i = 0; i < 4; ++i) PUTO(3, i);
    if (wid == 7) {
        const u64 tag = get_tag(ctl);
#pragma nounroll
        for (u32 sp = 0; sp < SPIN_CAP; ++sp) {
            if (__all(fy0 == tag && fy1 == tag && fy2 == tag && fy3 == tag)) break;
            TMO(sp, 2);
            __builtin_amdgcn_s_sleep(1);
            fy0 = get_flag(ws, WS_FY + lane * 8); fy1 = get_flag(ws, WS_FY + 512 + lane * 8); fy2 = get_flag(ws, WS_FY + 1024 + lane * 8); fy3 = get_flag(ws, WS_FY + 1536 + lane * 8);
        }
        if (blockIdx.x == 0) put_flag(ws, WS_EPOCH, tag, lane);
    }
    LGKM0(); __builtin_amdgcn_s_barrier(); asm volatile("" ::: "memory");
    const int c0 = 112 * cq + 16 * min(wid, 6) + 4 * (lane >> 4);
    u32x2 dk, xv[2]; u32 yv[2][4];
    dk = *(const u32x2*)(Dskip + c0 * 2);
#pragma unroll
    for (int r = 0; r < 2; ++r) {
        const int t = 32 * rt + 16 * r + (lane & 15);
        xv[r] = *(const u32x2*)(x + t * 896 + c0 * 2);
#pragma unroll
        for (int e = 0; e < 4; ++e) yv[r][e] = __builtin_amdgcn_raw_buffer_load_b16(rs, WS_YT + (u32)(c0 * 4096 + t * 2), e * 4096, 16);
    }
    SBAR();
    {
        f32x4 a0 = {}, a1 = {}; float l = 0.f;
#pragma unroll
        for (int w = 0; w < 8; ++w) {
            LAS char* const p = sm + w * 16384 + tw * 4096;
            a0 += *(LAS f32x4*)(p + team * 2048 + lane * 16); a1 += *(LAS f32x4*)(p + team * 2048 + 1024 + lane * 16);
            l += *(LAS float*)(p + 3072 + 512 + l31 * 16);
        }
        float mo = fmaxf(fmaxf(fmaxf(fabsf(a0[0]), fabsf(a0[1])), fmaxf(fabsf(a0[2]), fabsf(a0[3]))), fmaxf(fmaxf(fabsf(a1[0]), fabsf(a1[1])), fmaxf(fabsf(a1[2]), fabsf(a1[3]))));
        if (__builtin_expect(__any(!(l < 1.0e18f && l > 1.0e-12f && mo < 3.0e38f)), 0)) {
            const int o_ = (qt0 + tw) * QK_TILE; qf[0][0] = LDC128(qo, o_); qf[0][1] = LDC128(qo + 1024, o_); FIXQ(0);
            float m = -INFINITY;
#pragma nounroll
            for (int t = 0; t < 64; ++t) {
                f32x16 s = MFMA32(LDC128(ko, t * QK_TILE), qf[0][0], zero16);
                s = MFMA32(LDC128(ko + 1024, t * QK_TILE), qf[0][1], s);
#pragma unroll
                for (int r = 0; r < 16; ++r) m = fmaxf(m, s[r]);
            }
            m = fmaxf(m, __shfl_xor(m, 32));
            f32x16 o = zero16;
#pragma nounroll
            for (int t = 0; t < 64; ++t) {
                f32x16 s = MFMA32(LDC128(ko, t * QK_TILE), qf[0][0], zero16);
                s = MFMA32(LDC128(ko + 1024, t * QK_TILE), qf[0][1], s);
                const u32x4 a = LDC128(vo, t * V_TILE), b = LDC128(vo + 1024, t * V_TILE);
#pragma unroll
                for (int r = 0; r < 16; ++r) s[r] = __builtin_amdgcn_exp2f(s[r] - m);
                o = SMFMAC(a, LO8(s), o, ix); o = SMFMAC(b, HI8(s), o, ix);
            }
            l = __shfl(o[12], 32 + l31);
#pragma unroll
            for (int e = 0; e < 4; ++e) { a0[e] = team ? o[8 + e] : o[e]; a1[e] = team ? o[12 + e] : o[4 + e]; }
        }
        const float rl = __builtin_amdgcn_rcpf(l);
        u32x4 v = {pk_bf16(a0[0] * rl, a0[1] * rl), pk_bf16(a0[2] * rl, a0[3] * rl), pk_bf16(a1[0] * rl, a1[1] * rl), pk_bf16(a1[2] * rl, a1[3] * rl)};
#pragma unroll
        for (int e = 0; e < 4; ++e) v[e] = min(v[e], 0xfffffffeu);
        STC128(v, WS_O + (u32)((qt0 + tw) * 32768 + head * 2048 + team * 1024 + lane * 16));
    }
    STAMP(10);
    float g[2], bt[2][4];
    u32x4 a[4];
    const u32 oo = WS_O + (u32)(rt * 32768 + wid * 4096 + lane * 16);
#pragma unroll
    for (int i = 0; i < 4; ++i) a[i] = LDC128(oo + i * 1024, 0);
    SBAR();
#pragma unroll
    for (int r = 0; r < 2; ++r) {
        g[r] = *(LAS float*)(ctl + C_G + (16 * r + (lane & 15)) * 4);
        const float og = rbf(1.f - g[r]);
#pragma unroll
        for (int e = 0; e < 4; ++e) {
            const float xe = (e & 1) ? bf_hi(xv[r][e >> 1]) : bf_lo(xv[r][e >> 1]), de = (e & 1) ? bf_hi(dk[e >> 1]) : bf_lo(dk[e >> 1]);
            bt[r][e] = rbf(og * rbf(bf_lo(yv[r][e]) + rbf(xe * de)));
            PIN(bt[r][e]);
        }
    }
    LGKM0(); __builtin_amdgcn_s_barrier(); asm volatile("" ::: "memory");
#pragma nounroll
    for (u32 sp = 0; sp < SPIN_CAP; ++sp) {
        u32 mx = 0;
#pragma unroll
        for (int i = 0; i < 4; ++i) mx = max(max(mx, max(a[i][0], a[i][1])), max(a[i][2], a[i][3]));
        if (__all(mx != 0xffffffffu)) break;
        TMO(sp, 3);
        asm volatile("" ::: "memory");
#pragma unroll
        for (int i = 0; i < 4; ++i) a[i] = LDC128(oo + i * 1024, 0);
    }
#pragma unroll
    for (int i = 0; i < 4; ++i) *(LAS u32x4*)(sm + wid * 4096 + i * 1024 + lane * 16) = a[i];
    STAMP(11);
    LGKM0(); __builtin_amdgcn_s_barrier(); asm volatile("" ::: "memory");
    if (wid < 7) {
        f32x4 oa[2] = {};
        LAS char* const p = sm + (lane >> 4) * 512 + (lane & 15) * 16;
        u32x4 of[32];
#pragma unroll
        for (int h = 0; h < 32; ++h) of[h] = lds128(p + (h >> 1) * 2048 + (h & 1) * 256);
        SBAR();
#pragma unroll
        for (int h = 0; h < 32; ++h) oa[h & 1] = MFMA16(wof[h >> 1], of[h], oa[h & 1]);
#pragma unroll
        for (int r = 0; r < 2; ++r) {
            float v[4];
#pragma unroll
            for (int e = 0; e < 4; ++e) v[e] = rbf(g[r] * rbf(oa[r][e])) + bt[r][e];
            *(u32x2*)(out + (32 * rt + 16 * r + (lane & 15)) * 896 + c0 * 2) = (u32x2){pk_bf16(v[0], v[1]), pk_bf16(v[2], v[3])};
        }
    }

    STAMP(12);
}

extern "C" void kernel_launch(void* const* d_in, const int* in_sizes, int n_in,
                              void* d_out, int out_size, void* d_ws, size_t ws_size,
                              hipStream_t stream) {
    fused_kernel<<<256, 512, 0, stream>>>((const char*)d_in[0], (const char*)d_in[1], (const char*)d_in[2],
        (const char*)d_in[4], (const char*)d_in[5], (const char*)d_in[6], (const char*)d_in[7], (const char*)d_in[8], (const char*)d_in[9],
        (const char*)d_in[10], (const char*)d_in[11], (const char*)d_in[12], (char*)d_out, (char*)d_ws);
}
```

```cpp
#include <hip/hip_runtime.h>
#include <stdint.h>

typedef unsigned short u16;
typedef unsigned int u32;
typedef unsigned long long u64;
typedef float f32x2 __attribute__((ext_vector_type(2)));
typedef float f32x4 __attribute__((ext_vector_type(4)));
typedef float f32x8 __attribute__((ext_vector_type(8)));
typedef float f32x16 __attribute__((ext_vector_type(16)));
typedef u32 u32x2 __attribute__((ext_vector_type(2)));
typedef u32 u32x4 __attribute__((ext_vector_type(4)));
typedef short s16x4 __attribute__((ext_vector_type(4)));
typedef __bf16 bf16x2 __attribute__((ext_vector_type(2)));
typedef __bf16 bf16x8 __attribute__((ext_vector_type(8)));
typedef __bf16 bf16x16 __attribute__((ext_vector_type(16)));
#define LAS __attribute__((address_space(3)))
#define GAS __attribute__((address_space(1)))
#define DEV __device__ __forceinline__
template <bool B> struct BoolC { static constexpr bool value = B; };
template <int I> struct IntC { static constexpr int value = I; };
template <int I, int N, class F> DEV void static_for(F&& f) { if constexpr (I < N) { f(IntC<I>{}); static_for<I + 1, N>(f); } }

constexpr u32 WS_EPOCH = 0;
constexpr u32 WS_FT = 0x20000;
constexpr u32 WS_FA = 0x50000;
constexpr u32 WS_FW = 0x48000;
constexpr u32 WS_FY = 8192;
constexpr u32 WS_DBG = 0x90000;
constexpr u32 WS_QK = 1u << 20;
constexpr u32 QK_TILE = 112 * 512;
constexpr u32 WS_V = WS_QK + 64 * QK_TILE;
constexpr u32 V_TILE = 32768;
constexpr u32 WS_O = WS_V + 64 * V_TILE;
constexpr u32 WS_XT = WS_O + 64 * 32768;
constexpr u32 WS_YT = WS_XT + 448 * 4096;
constexpr u32 WS_WOF = WS_YT + 448 * 4096;
constexpr u32 WS_END = WS_WOF + 28 * 16384;

constexpr u32 L_B = 0;
constexpr u32 L_X = 65536;
constexpr u32 L_W = L_X + 3 * 12288;
constexpr u32 L_TR = L_W + 3 * 16384;
constexpr u32 L_A = 65536;
constexpr u32 L_OS = 161280;
constexpr u32 L_XR0 = 49152, L_XR1 = 131072;
constexpr u32 L_GW = 145408, L_DK = L_GW + 1024;
constexpr u32 L_Q = 147456;
constexpr u32 L_O2 = 16384;
constexpr u32 L_PF = 161792;
constexpr u32 L_CTL = 162816;
constexpr u32 C_A = 0, C_B = 4, C_P0 = 8, C_KV = 16, C_XT = 20, C_M = 24, C_FY = 28, C_H0 = 32, C_Y = 52, C_L = 56, C_G = 64;
constexpr u32 B_BIG = 0, B_KR = 16640, B_T = 24064, B_SCR = 4352, B_UC = 4160;
constexpr u32 SPIN_CAP = 1u << 14;
#define POLL_SLEEP 3
#define FIRST_SLEEP 4
#define KG_SCALAR 0
#define B_START 28
#define OSLEEP "2"
#define O_FIRST 3
#define A_PRIO 2
#define K_PRIO 0
#define L2PF 0
#define PFC 4
#define PFW "0"
#define BP_END 18
#define FY_STEP 13
#define B_PRE 4
#define XT_START 16
#define CONV_PRIO 1

#define MFMA32(a, b, c) __builtin_amdgcn_mfma_f32_32x32x16_bf16(__builtin_bit_cast(bf16x8, a), __builtin_bit_cast(bf16x8, b), c, 0, 0, 0)
#define MFMA16(a, b, c) __builtin_amdgcn_mfma_f32_16x16x32_bf16(__builtin_bit_cast(bf16x8, a), __builtin_bit_cast(bf16x8, b), c, 0, 0, 0)
#define SMFMAC(a, p8, c, ix) __builtin_amdgcn_smfmac_f32_32x32x32_bf16(__builtin_bit_cast(bf16x8, a), __builtin_bit_cast(bf16x16, p8), c, ix, 0, 0)
#define SBAR() __builtin_amdgcn_sched_barrier(0)
#define PIN(x) asm volatile("" : "+v"(x))
#define VMCNT(n) asm volatile("s_waitcnt vmcnt(" #n ")" ::: "memory")
#define LGKM0() asm volatile("s_waitcnt lgkmcnt(0)" ::: "memory")
#define RLX_AGENT __ATOMIC_RELAXED, __HIP_MEMORY_SCOPE_AGENT
#define RLX_WG __ATOMIC_RELAXED, __HIP_MEMORY_SCOPE_WORKGROUP
#define LDC128(off, soff) __builtin_amdgcn_raw_buffer_load_b128(rs, off, soff, 16)
#define STC128(v, off) __builtin_amdgcn_raw_buffer_store_b128(v, rs, off, 0, 16)

DEV u32 pk_bf16(float lo, float hi) { f32x2 v = {lo, hi}; return __builtin_bit_cast(u32, __builtin_convertvector(v, bf16x2)); }
DEV float bf_lo(u32 v) { return __uint_as_float(v << 16); }
DEV float bf_hi(u32 v) { return __uint_as_float(v & 0xffff0000u); }
DEV float rbf(float f) { return bf_lo(pk_bf16(f, f)); }
DEV float dot2(u32 a, u32 b, float c) { return __builtin_amdgcn_fdot2_f32_bf16(__builtin_bit_cast(bf16x2, a), __builtin_bit_cast(bf16x2, b), c, false); }
DEV int rfl(int v) { return __builtin_amdgcn_readfirstlane(v); }
DEV void set_m0(u32 v) { asm volatile("s_mov_b32 m0, %0\n\ts_nop 0" :: "s"(v) : "memory"); }
template <int OFF> DEV void dma16(u32 voff, const void* sbase) {
    asm volatile("global_load_lds_dwordx4 %0, %1 offset:%2" :: "v"(voff), "s"(sbase), "i"(OFF) : "memory"); }
template <int OFF> DEV void put_tid(u32 v) { asm volatile("ds_write_addtid_b32 %0 offset:%1" :: "v"(v), "i"(OFF) : "memory"); }
DEV u32x2 lds_tr(LAS char* p) { return __builtin_bit_cast(u32x2, __builtin_amdgcn_ds_read_tr16_b64_v4i16((LAS s16x4*)p)); }
DEV u32x4 lds128(LAS char* p) { return *(LAS u32x4*)p; }
DEV u32x4 cat(u32x2 a, u32x2 b) { return (u32x4){a[0], a[1], b[0], b[1]}; }
DEV void arrive(LAS char* c) {
    asm volatile("s_mov_b64 exec, 1\n\tds_add_u32 %0, %1\n\ts_mov_b64 exec, -1" :: "v"((u32)(uintptr_t)c), "v"(1u) : "memory"); }
DEV u32 arrive_rtn(LAS char* c) {
    u32 r, s;
    asm volatile("s_mov_b64 exec, 1\n\tds_add_rtn_u32 %0, %2, %3\n\ts_waitcnt lgkmcnt(0)\n\tv_readfirstlane_b32 %1, %0\n\ts_mov_b64 exec, -1\n\ts_nop 3"
                 : "=&v"(r), "=s"(s) : "v"((u32)(uintptr_t)c), "v"(1u) : "memory");
    return s; }
DEV void wait_ge(LAS char* c, u32 target) {
    u32 r, s, n;
    asm volatile("s_mov_b32 %2, 0x400000\n"
                 "1:\n\tds_read_b32 %0, %4\n\ts_sub_u32 %2, %2, 1\n\ts_cselect_b32 %3, 0, %3\n\ts_waitcnt lgkmcnt(0)\n\tv_readfirstlane_b32 %1, %0\n\t"
                 "s_cmp_lt_u32 %1, %3\n\ts_cbranch_scc1 1b"
                 : "=&v"(r), "=&s"(s), "=&s"(n), "+s"(target) : "v"((u32)(uintptr_t)c) : "memory", "scc"); }
DEV void put_flag(char* ws, u32 off, u64 tag, int lane) { if (lane == 0) __hip_atomic_store((GAS u64*)(ws + off), tag, RLX_AGENT); }
DEV u64 get_flag(char* ws, u32 off) { return __hip_atomic_load((GAS u64*)(ws + off), RLX_AGENT); }
#define CMUL_PK 1
DEV f32x2 cmul(f32x2 a, f32x2 w) {
    f32x2 r;
    asm("v_pk_mul_f32 %0, %1, %2 op_sel_hi:[0,1]\n\tv_pk_fma_f32 %0, %1, %2, %0 op_sel:[1,1,0] op_sel_hi:[1,0,1] neg_lo:[0,1,0]" : "=&v"(r) : "v"(a), "v"(w));
    return r;
}
DEV float smul(float a, float b) { float r; asm("v_mul_f32 %0, %1, %2" : "=v"(r) : "v"(a), "v"(b)); return r; }
DEV float sadd(float a, float b) { float r; asm("v_add_f32 %0, %1, %2" : "=v"(r) : "v"(a), "v"(b)); return r; }
DEV float sfma(float a, float b, float c) { float r; asm("v_fma_f32 %0, %1, %2, %3" : "=v"(r) : "v"(a), "v"(b), "v"(c)); return r; }
DEV float nmul(float a, float b) { float r = a * b; asm("" : "+v"(r)); return r; }
DEV float nadd(float a, float b) { float r = a + b; asm("" : "+v"(r)); return r; }
DEV bool poll_flags(char* ws, u32 off, u64 tag, bool seen) {
    if (!seen) seen = get_flag(ws, off) == tag;
    return seen; }
DEV f32x2 zpow(float al, float be, float g) {
    const float m = __builtin_amdgcn_exp2f(al * g), a = __builtin_amdgcn_fractf(be * g);
    return (f32x2){m * __builtin_amdgcn_cosf(a), m * __builtin_amdgcn_sinf(a)}; }

#define STAMP(k) do {} while (0)
#define TMO(sp, k) do {} while (0)

#define POLL256(off, id) _Pragma("nounroll") for (u32 sp = 0; sp < SPIN_CAP; ++sp) { \
        const u32x4 a_ = LDC128(off, 0), b_ = LDC128((off) + 1024, 0); const u32 lo_ = (u32)tag, hi_ = (u32)(tag >> 32); \
        if (__all((((a_[0] ^ lo_) | (a_[2] ^ lo_) | (b_[0] ^ lo_) | (b_[2] ^ lo_)) | ((a_[1] ^ hi_) | (a_[3] ^ hi_) | (b_[1] ^ hi_) | (b_[3] ^ hi_))) == 0u)) break; \
        TMO(sp, id); asm volatile("s_sleep 8" ::: "memory"); }
struct Tab { u32 e[256]; };

__global__ void __launch_bounds__(512, 2)
fused_kernel(const char* __restrict__ x, const char* __restrict__ wqkv, const char* __restrict__ wo,
             const char* __restrict__ pAre, const char* __restrict__ pAim, const char* __restrict__ pBre, const char* __restrict__ pBim,
             const char* __restrict__ pCre, const char* __restrict__ pCim, const char* __restrict__ logdt, const char* __restrict__ Dskip,
             const char* __restrict__ gatew, char* __restrict__ out, char* ws, const Tab tab) {
    __shared__ __attribute__((aligned(256))) char smem_[163840];
    LAS char* const sm = (LAS char*)smem_;
    const u32 sm0 = (u32)(uintptr_t)sm;
    LAS char* const ctl = sm + L_CTL;
    STAMP(0);
    u64 pc0; asm volatile("s_getpc_b64 %0\n.Lcode_begin:" : "=s"(pc0));
    const int lane = threadIdx.x & 63, wid = rfl(threadIdx.x >> 6), team = wid >> 2, tw = wid & 3;
    const int xcd = blockIdx.x & 7, j = blockIdx.x >> 3;
    const int l31 = lane & 31, hh = lane >> 5;
    const u32 te = tab.e[blockIdx.x];
    const int pw = tw & 1, ch = 56 * xcd + 2 * min(j, 27) + (tw >> 1);
    u64 ep;
    *(LAS u32*)(ctl + (lane & 15) * 4) = 0u;
    u32 o0 = (u32)(ch * 512 + pw * 256 + lane * 2);
    u32 a0_ = (u32)((16 * tw + (lane >> 4)) * 2688 + (((lane & 15) ^ ((lane >> 4) << 2)) << 4));
    u32 a1_ = (u32)((24 * tw + (lane >> 3)) * 896 + (((lane & 7) ^ (((tw & 1) << 2) | (lane >> 4))) << 4));
    const int trb = 4 * (xcd < 4 ? 2 * xcd + 1 : 2 * xcd - 8) + (j & 3), tc = min(j >> 2, 6);
    u32 o1 = (u32)((64 * trb + 16 * tw + (lane >> 3)) * 896 + tc * 128 + (lane & 7) * 16);
    PIN(o0); PIN(o1); PIN(a0_); PIN(a1_);
#define LOAD_EPOCH(WAIT) asm volatile("global_load_dwordx2 %0, %1, %2 sc1 ; epoch" WAIT : "=&v"(ep) : "v"(0u), "s"(ws) : "memory")
#define EPOCH_IN(N) asm volatile("s_waitcnt vmcnt(" #N ") ; parameters landed" : "+v"(ep) :: "memory")
    asm volatile("" :: "s"(ws), "s"(x), "s"(wqkv), "s"(te), "s"(wo), "s"(Dskip), "s"(gatew), "s"(out),
                 "s"(pAre), "s"(pAim), "s"(pBre), "s"(pBim), "s"(pCre), "s"(pCim), "s"(logdt) : "memory");
    const int t0 = te & 255, ct = ((te >> 8) & 255) + tw;
    const bool qv = (te >> 31) != 0, isV = ct >= 28;
    const bool sv = j < 28;
    const __amdgpu_buffer_rsrc_t rs = __builtin_amdgcn_make_buffer_rsrc(ws, 0, (int)WS_END, 0x00020000);
    const int head = 2 * xcd + (j >> 4), qt0 = 4 * (j & 15);
    const int G8 = (7 * head) >> 1;
    const int rt = 8 * xcd + (j >> 2), cq = j & 3;
    u32 cnt = 0;
    u64 tag;
#define GET_TAG() tag = (((u64)(u32)rfl((int)(ep >> 32)) << 32) | (u32)rfl((int)ep)) + 0x9E3779B97F4A7C15ull
    LAS char* const pc = ctl + C_P0 + (tw >> 1) * 4;
    LAS char* const cb_ = sm + L_B + (tw >> 1) * 32768;
    const u32 fa = WS_FA + (ct * 32 + ((te >> 16) & 31)) * 128;
#define STAMPX(k) STAMP(k)
#define STAMPQ(k)
#define STAMPNQ(k) STAMP(k)
    u32x2 yq[4]; u32 yo = 0xf0000000u;
    u32 l16, m0_, m1_, ix, part, from, fo;
    auto att_setup = [&]() {
        l16 = lane * 16; ix = 0xddddddddu;
        m0_ = ((head & 1) && !hh) ? 0u : ~0u; m1_ = (!(head & 1) && hh) ? 0u : ~0u;
        part = L_A + tw * 16384 + l16;
        from = team ? part : L_OS + tw * 16;
        const int kind = (lane >> 3) & 3, tile = lane < 32 ? 8 * wid + (lane & 7) : qt0 + ((tw + lane) & 3);
        const int col = lane < 32 ? (kind < 2 ? 448 + 8 * G8 + 31 * kind : 896 + 28 * head + 27 * (kind - 2)) : 8 * G8 + 31 * ((lane >> 2) & 1);
        fo = lane < 40 ? WS_FA + ((col >> 5) * 32 + ((tile * 43) >> 7)) * 128 : WS_FW + (7 * cq + min(lane, 46) - 40) * 128;
        PIN(l16); PIN(ix); PIN(m0_); PIN(m1_); PIN(part); PIN(from); PIN(fo);
    };
    auto zero_area = [&]() {
        wait_ge(ctl + C_A, cnt);
        *(LAS u32x4*)(sm + L_OS + tw * 16) = (u32x4){0u, 0u, 0u, 0u};
    };
    if (!qv) {
        const u32 o = (u32)((lane >> 3) * 896 + (lane & 7) * 16);
        LOAD_EPOCH("\n\ts_waitcnt vmcnt(0) lgkmcnt(0)\n\ts_barrier");
        GET_TAG();
        if (!team) { arrive(ctl + C_A); cnt += 4; zero_area(); att_setup(); }
        if (j == 31 && team) {
            const int u = (te >> 17) & 7;
#pragma nounroll
            for (int i = tw; i < 56; i += 4) { set_m0(sm0 + i * 1024); dma16<0>(o, wo + i * (8 * 896) + u * 128); }
            VMCNT(0); arrive(ctl + C_B); cnt += 4; wait_ge(ctl + C_B, cnt);
            const int g = lane >> 4;
            LAS char* q = sm + (16 * (g >> 1) + 4 * (g & 1) + ((lane & 15) >> 2)) * 128 + tw * 32 + (lane & 3) * 8;
            const u32 mk = g == 3 ? 0u : ~0u;
#pragma nounroll
            for (int h4 = 0; h4 < 16; h4 += 4) {
                u32x2 a[4], b[4];
#pragma unroll
                for (int h = 0; h < 4; ++h) { a[h] = lds_tr(q + (h4 + h) * (28 * 128)); b[h] = lds_tr(q + (h4 + h) * (28 * 128) + 1024); }
                SBAR();
#pragma unroll
                for (int h = 0; h < 4; ++h) { PIN(b[h]); STC128(((u32x4){a[h][0], a[h][1], b[h][0] & mk, b[h][1] & mk}), WS_WOF + (u32)(((4 * u + tw) * 16 + h4 + h) * 1024 + lane * 16)); }
            }
            VMCNT(0);
            put_flag(ws, WS_FW + (4 * u + tw) * 128, tag, lane);
        }
    } else if (team == 0) {
        u32 pre[16];
#define OX(i) pre[i]
#define OW(i) pre[3 + (i)]
#define XA(i) pre[7 + (i)]
#define WA pre[11]
#define OB pre[12]
        const char* const xb = x + t0 * (32 * 896); const char* const wb = wqkv + ((te >> 8) & 255) * 64;
#define W_IN(c) do { set_m0(sm0 + L_W + (c) * 16384 + tw * 4096); const char* wc_ = wb + (c) * (64 * 2688); \
                     dma16<0>(OW(0), wc_); dma16<1024>(OW(1), wc_); dma16<2048>(OW(2), wc_); dma16<3072>(OW(3), wc_); } while (0)
#define XS(s) ((((s) + 2) % 3) * 12288)
#define X_IN(c) do { set_m0(sm0 + L_X + XS(c) - (c) * 128 + tw * 3072); \
                     dma16<(c) * 128>(OX(0), xb); dma16<1024 + (c) * 128>(OX(1), xb); dma16<2048 + (c) * 128>(OX(2), xb); } while (0)
        OW(0) = a0_; OX(0) = a1_;
#pragma unroll
        for (int i = 1; i < 4; ++i) OW(i) = OW(0) + i * (4 * 2688 - 1024);
        OX(1) = (OX(0) ^ 64u) + (8 * 896 - 1024); OX(2) = OX(0) + (16 * 896 - 2048);
        W_IN(0); X_IN(0);
        asm volatile("s_waitcnt lgkmcnt(0)\n\ts_barrier" ::: "memory"); SBAR();
        LOAD_EPOCH("");
        const u32 ts = isV ? V_TILE : QK_TILE;
        {
            const int sw = (l31 & 19) | ((l31 & 4) << 1) | ((l31 & 8) >> 1);
            const int xr = isV ? sw : l31, p2 = lane & 3, pp = isV ? p2 : (((p2 & 1) << 1) | (p2 >> 1)), q = (lane & 15) >> 2;
#pragma unroll
            for (int ks = 0; ks < 4; ++ks) XA(ks) = (u32)(L_X + xr * 128 + (((2 * ks + hh) ^ ((xr >> 1) & 7)) << 4));
            WA = (u32)(L_W + (8 * hh + q) * 256 + ((tw ^ q) << 6) + ((lane >> 4) & 1) * 32 + pp * 8);
            const int vn = (ct - 28) * 32 + l31, vh = (vn * 2341) >> 16, vd = vn - 28 * vh;
            OB = isV ? WS_V + (u32)(vh * 2048 + hh * 512 + vd * 16 + t0 * V_TILE) : WS_QK + (u32)(ct * 2048 + lane * 16 + t0 * QK_TILE);
            pre[13] = (u32)(uintptr_t)(ctl + C_A); pre[14] = 1u; pre[15] = (u32)vd;
#pragma unroll
            for (int i = 0; i < 16; ++i) PIN(pre[i]);
        }
        u32 ft = WS_FT + (u32)(((ch >> 4) * 32 + l31) * 128), xo = WS_XT + (u32)(ch * 4096 + (128 * pw + lane) * 16), xp = (pw == 0 && lane == 0) ? 0xf0000000u : xo - 8;
        PIN(ft); PIN(xo); PIN(xp);
        W_IN(1); X_IN(1);
        __builtin_amdgcn_s_setprio(A_PRIO);
        STAMP(1);
#define ARRIVE_A() asm volatile("s_mov_b64 exec, 1\n\tds_add_u32 %0, %1\n\ts_mov_b64 exec, -1" :: "v"(pre[13]), "v"(pre[14]) : "memory")
        EPOCH_IN(7); ARRIVE_A(); cnt += 4;
        W_IN(2);
        GET_TAG();
        wait_ge(ctl + C_A, cnt);
        STAMP(2);
        u32x4 ud[2]; u32x2 up[2]; bool xt_ok;
#define LDXT() do { ud[0] = LDC128(xo, 0); ud[1] = LDC128(xo + 1024, 0); up[0] = __builtin_amdgcn_raw_buffer_load_b64(rs, xp, 0, 16); up[1] = __builtin_amdgcn_raw_buffer_load_b64(rs, xo + 1016, 0, 16); } while (0)
        auto copies = [&]() {
            if (lane < 20) *(LAS u32x4*)(cb_ + (lane < 16 ? B_KR + 4096 + (pw * 16 + lane) * 16 : B_BIG + (lane - 16) * B_UC + pw * 16)) = (u32x4){0u, 0u, 0u, 0u};
#pragma unroll
            for (int k = 0; k < 2; ++k) {
                LAS char* const d = cb_ + B_BIG + (128 * pw + 64 * k + lane + 2) * 16;
                const u32 a0 = __builtin_amdgcn_alignbit(up[k][1], up[k][0], 16), a1 = __builtin_amdgcn_alignbit(ud[k][0], up[k][1], 16),
                          a2 = __builtin_amdgcn_alignbit(ud[k][1], ud[k][0], 16), a3 = __builtin_amdgcn_alignbit(ud[k][2], ud[k][1], 16),
                          a4 = __builtin_amdgcn_alignbit(ud[k][3], ud[k][2], 16);
                *(LAS u32x4*)(d) = ud[k];
                *(LAS u32x4*)(d + B_UC) = (u32x4){a1, a2, a3, a4};
                *(LAS u32x4*)(d + 2 * B_UC) = (u32x4){up[k][1], ud[k][0], ud[k][1], ud[k][2]};
                *(LAS u32x4*)(d + 3 * B_UC) = (u32x4){a0, a1, a2, a3};
            }
            arrive(ctl + C_XT + (tw >> 1) * 28);
        };
        {
            f32x16 acc[3];
            u32x2 fl;
            float sc = ct < 14 ? 0.18898223650461362f * 1.4426950408889634f : 1.0f; PIN(sc);
            auto kloop = [&](auto vo) {
                u32x4 wf[4], xf[4][3];
#define RDW(k, s) wf[k] = cat(lds_tr(sm + WA + (s) * 16384 + (k) * 4096), lds_tr(sm + WA + (s) * 16384 + (k) * 4096 + 1024))
#define RDX(k, i, s) xf[k][i] = lds128(sm + XA(k) + XS(s) + (i) * 4096)
#define MMC(k, i, cin) do { SBAR(); if constexpr (decltype(vo)::value) acc[i] = MFMA32(xf[k][i], wf[k], cin); else acc[i] = MFMA32(wf[k], xf[k][i], cin); SBAR(); } while (0)
#define MM(k, i) MMC(k, i, acc[i])
                RDW(0, 0); RDX(0, 0, 0); RDX(0, 1, 0); RDX(0, 2, 0); RDW(1, 0); RDX(1, 0, 0); RDX(1, 1, 0); RDX(1, 2, 0); RDW(2, 0); RDX(2, 0, 0);
                static_for<0, 6>([&](auto cc) {
                    constexpr int c = decltype(cc)::value, sl = c % 3, sn = (c + 1) % 3, sp = (c + 2) % 3;
                    constexpr bool LW = c <= 3, LX = c <= 4;
                    const char* wc = wb + (c + 3) * (64 * 2688);
                    if constexpr (c == 0) STAMPQ(3); if constexpr (c == 1) STAMPQ(4); if constexpr (c == 2) STAMPQ(5); if constexpr (c == 3) STAMPQ(13); if constexpr (c == 4) STAMPQ(1); if constexpr (c == 5) STAMPQ(10); if constexpr (c == 6) STAMPQ(11);
                    if constexpr (c > 0) MM(0, 0); else MMC(0, 0, ((f32x16){}));
                    RDX(2, 1, sl); RDX(2, 2, sl); if constexpr (LX) { set_m0(sm0 + L_X + XS(sp) + tw * 3072 - (c + 2) * 128); dma16<(c + 2) * 128>(OX(0), xb); }
                    if constexpr (c > 0) MM(0, 1); else MMC(0, 1, ((f32x16){}));
                    RDW(3, sl); RDX(3, 0, sl); if constexpr (LX) dma16<1024 + (c + 2) * 128>(OX(1), xb);
                    if constexpr (c > 0) MM(0, 2); else MMC(0, 2, ((f32x16){}));
                    RDX(3, 1, sl); RDX(3, 2, sl); if constexpr (LX) dma16<2048 + (c + 2) * 128>(OX(2), xb);
                    MM(1, 0);
                    if constexpr (c <= 4) VMCNT(7); else if constexpr (c == 5) VMCNT(0);
                    if constexpr (c < 6) { ARRIVE_A(); cnt += 4; }

                    MM(1, 1);

                    if constexpr (c == 4) fl = __builtin_amdgcn_raw_buffer_load_b64(rs, ft, 0, 16);
                    if constexpr (c == 5) xt_ok = __all(fl[0] == (u32)tag && fl[1] == (u32)(tag >> 32));
                    MM(1, 2);
                    u32 cv = 0;
                    cv = *(volatile LAS u32*)(ctl + C_A);

                    MM(2, 0);
                    if constexpr (c == 5) if (xt_ok) LDXT();
                    if constexpr (LW) set_m0(sm0 + L_W + sl * 16384 + tw * 4096);

                    MM(2, 1);
                    if constexpr (c < 6) {
                        if (__builtin_expect((u32)rfl((int)cv) < cnt, 0)) wait_ge(ctl + C_A, cnt);
                        RDW(0, sn);
                    }
                    MM(2, 2);
                    if constexpr (c < 6) { RDX(0, 0, sn); RDX(0, 1, sn); } if constexpr (LW) dma16<0>(OW(0), wc);
                    MM(3, 0);
                    if constexpr (c < 6) { RDX(0, 2, sn); RDW(1, sn); } if constexpr (LW) dma16<1024>(OW(1), wc);
                    MM(3, 1);
                    if constexpr (c < 6) { RDX(1, 0, sn); RDX(1, 1, sn); } if constexpr (LW) dma16<2048>(OW(2), wc);

                    MM(3, 2);
                    if constexpr (c < 6) { RDX(1, 2, sn); RDW(2, sn); RDX(2, 0, sn); } if constexpr (LW) dma16<3072>(OW(3), wc);
                });
                STAMPQ(11);
                auto out_half = [&](int i, int s) {
                    u32x4 v;
#pragma unroll
                    for (int e = 0; e < 4; ++e) {
                        if constexpr (decltype(vo)::value) v[e] = pk_bf16(acc[i][8 * s + 2 * e], acc[i][8 * s + 2 * e + 1]);
                        else v[e] = pk_bf16(nmul(acc[i][8 * s + 2 * e], sc), nmul(acc[i][8 * s + 2 * e + 1], sc));
                    }
                    STC128(v, OB + i * ts + s * 1024);
                };
                MM(0, 0); RDW(3, 0); RDX(3, 0, 0);
                MM(1, 0); RDX(2, 1, 0); RDX(3, 1, 0);
                MM(2, 0); RDX(2, 2, 0); RDX(3, 2, 0);
                MM(0, 1); ARRIVE_A(); cnt += 4;
                MM(3, 0);
                MM(1, 1);
                MM(2, 1); out_half(0, 0);
                MM(3, 1); out_half(0, 1);
                MM(0, 2);
                MM(1, 2); out_half(1, 0);
                MM(2, 2); out_half(1, 1);
                MM(3, 2);
                if constexpr (decltype(vo)::value) if (pre[15] >= 24u) {
                    const u32 one = pre[15] == 24u ? 0x3f803f80u : 0u;
#pragma unroll
                    for (int i = 0; i < 6; ++i) STC128(((u32x4){one, one, one, one}), OB + (i >> 1) * V_TILE + (i & 1) * 1024 + 64);
                }
                out_half(2, 0); out_half(2, 1);
            };
            if (isV) kloop(BoolC<true>{}); else kloop(BoolC<false>{});
            STAMPX(3); STAMPQ(8);
            __builtin_amdgcn_s_setprio(2);
            if (xt_ok && sv) copies();
            STAMPX(13);
            VMCNT(0); put_flag(ws, fa, tag, lane);
            STAMPX(5);
            zero_area(); att_setup();
            if (__builtin_expect(!xt_ok && sv, 0)) {
                bool seen = hh;
#pragma nounroll
                for (u32 sp = 0; sp < SPIN_CAP; ++sp) {
                    seen = poll_flags(ws, ft, tag, seen);
                    if (__all(seen)) break;
                    TMO(sp, 1);
                    __builtin_amdgcn_s_sleep(POLL_SLEEP);
                }
                asm volatile("" ::: "memory");
                LDXT();
                copies();
            } else __builtin_amdgcn_s_sleep(FIRST_SLEEP);
        }
        STAMPX(4);
    } else {
        set_m0(sm0 + L_TR + tw * 2048);
        dma16<0>(o1, x); dma16<1024>(o1 + 8 * 896 - 1024, x);
        u32 pa_[2][6], vldt = 0u, pre[16];
#pragma unroll
        for (int i = 0; i < 6; ++i) { pa_[0][i] = 0u; pa_[1][i] = 0u; }
        asm volatile("global_load_short_d16_hi %6, %14, %15\n\tglobal_load_short_d16_hi %0, %7, %8\n\tglobal_load_short_d16_hi %1, %7, %9\n\t"
                     "global_load_short_d16_hi %2, %7, %10\n\tglobal_load_short_d16_hi %3, %7, %11\n\tglobal_load_short_d16_hi %4, %7, %12\n\tglobal_load_short_d16_hi %5, %7, %13"
                     : "+&v"(pa_[0][0]), "+&v"(pa_[0][1]), "+&v"(pa_[0][2]), "+&v"(pa_[0][3]), "+&v"(pa_[0][4]), "+&v"(pa_[0][5]), "+&v"(vldt)
                     : "v"(o0), "s"(pAre), "s"(pAim), "s"(pBre), "s"(pBim), "s"(pCre), "s"(pCim), "v"((u32)(ch * 2)), "s"(logdt) : "memory");
        asm volatile("global_load_short_d16_hi %0, %6, %7 offset:128\n\tglobal_load_short_d16_hi %1, %6, %8 offset:128\n\tglobal_load_short_d16_hi %2, %6, %9 offset:128\n\t"
                     "global_load_short_d16_hi %3, %6, %10 offset:128\n\tglobal_load_short_d16_hi %4, %6, %11 offset:128\n\tglobal_load_short_d16_hi %5, %6, %12 offset:128"
                     : "+&v"(pa_[1][0]), "+&v"(pa_[1][1]), "+&v"(pa_[1][2]), "+&v"(pa_[1][3]), "+&v"(pa_[1][4]), "+&v"(pa_[1][5])
                     : "v"(o0), "s"(pAre), "s"(pAim), "s"(pBre), "s"(pBim), "s"(pCre), "s"(pCim) : "memory");
        LOAD_EPOCH("");
        if (lane < 2) {
            u32 len;
            asm volatile("s_mov_b32 %0, .Lcode_end-.Lcode_begin" : "=s"(len));
            set_m0(sm0 + L_PF);
            asm volatile("global_load_lds_dword %0, %1" :: "v"(min((u32)((j + 30 * (2 * tw + lane)) * 128), len)), "s"(pc0) : "memory");
        }
        asm volatile("s_waitcnt lgkmcnt(0)\n\ts_barrier" ::: "memory"); SBAR();
        LAS char* const scr = cb_ + B_T + pw * B_SCR;
        float lf0 = (float)(2047 - 1024 * pw - 2 * lane); PIN(lf0);
#pragma unroll
        for (int k = 0; k < 8; ++k) {
            const u32 b = pk_bf16(lf0 - 128.f * k, lf0 - (128.f * k + 1.f));
            const int l0 = 2047 - 2 * (512 * pw + 64 * k + lane);
            const u32 tb_ = (u32)(uintptr_t)cb_ - 0x4280u * 4u;
            pre[2 * k] = tb_ + (k < 7 ? (b & 0xffffu) : max(b & 0xffffu, (u32)(min(l0, 128) + 0x4280))) * 4u;
            pre[2 * k + 1] = tb_ + (k < 7 ? (b >> 16) : max(b >> 16, (u32)(min(l0 - 1, 128) + 0x4280))) * 4u;
        }
#pragma unroll
        for (int i = 0; i < 16; ++i) PIN(pre[i]);
        u32 fr = (u32)(uintptr_t)(scr + (lane & 15) * 272 + (lane >> 4) * 16);
        f32x4 ka[3] = {};
        u32 o2 = WS_XT + (u32)((64 * tc + 16 * tw + (lane & 15)) * 4096 + trb * 128 + (lane >> 4) * 16);
        u32 trp = (u32)(L_TR + (8 * (lane >> 4) + ((lane & 15) >> 2)) * 128 + tw * 32 + (lane & 3) * 8);
        PIN(o2); PIN(trp); PIN(fr); PIN(ka[0]); PIN(ka[1]); PIN(ka[2]);
        asm volatile("s_waitcnt vmcnt(0) ; parameters landed" : "+v"(pa_[0][0]), "+v"(pa_[0][1]), "+v"(pa_[0][2]), "+v"(pa_[0][3]), "+v"(pa_[0][4]), "+v"(pa_[0][5]),
                     "+v"(pa_[1][0]), "+v"(pa_[1][1]), "+v"(pa_[1][2]), "+v"(pa_[1][3]), "+v"(pa_[1][4]), "+v"(pa_[1][5]), "+v"(vldt), "+v"(ep) :: "memory");
        arrive(ctl + C_B); cnt += 4;
        GET_TAG();
        STAMP(1);
        if (sv) {
            const float ld = __uint_as_float(vldt), eh = ld * 1.4426950408889634f;
            const float el = __builtin_fmaf(ld, 1.925963033500011e-8f, __builtin_fmaf(ld, 1.4426950408889634f, -eh));
            const float e0 = __builtin_amdgcn_exp2f(eh);
            float dt = rbf(__builtin_fmaf(e0, el * 0.6931471805599453f, e0)); PIN(dt);
            wait_ge(ctl + C_B, cnt);
            STC128(cat(lds_tr(sm + trp), lds_tr(sm + trp + 512)), o2);
            STC128(cat(lds_tr(sm + trp + 4096), lds_tr(sm + trp + 4608)), o2 + 64);
            set_m0((u32)(uintptr_t)scr);
            __builtin_amdgcn_s_setprio(K_PRIO);
#pragma nounroll
            for (int r = 0; r < 2; ++r) {
                asm volatile(
                "v_mul_f32 v164, %[dt], %[ar]\n\t"
                "v_mul_f32 v165, %[dt], %[ai]\n\t"
                "v_mul_f32 v128, %[cr], %[br]\n\t"
                "v_mul_f32 v164, 0x3fb8aa3b, v164\n\t"
                "v_mul_f32 v165, 0.15915494, v165\n\t"
                "v_mul_f32 v129, %[cr], %[bi]\n\t"
                "v_mul_f32 v161, 0x41000000, v165\n\t"
                "v_mul_f32 v160, 0x41000000, v164\n\t"
                "v_fract_f32 v161, v161\n\t"
                "v_exp_f32 v160, v160\n\t"
                "v_cos_f32 v148, v161\n\t"
                "v_sin_f32 v149, v161\n\t"
                "v_fract_f32 v163, -v165\n\t"
                "v_exp_f32 v162, v164\n\t"
                "v_mul_f32 v148, v160, v148\n\t"
                "v_mul_f32 v149, v160, v149\n\t"
                "v_cos_f32 v146, v163\n\t"
                "v_sin_f32 v147, v163\n\t"
                "v_mul_f32 v161, 0xc3000000, v165\n\t"
                "v_mul_f32 v160, 0x43000000, v164\n\t"
                "v_fract_f32 v161, v161\n\t"
                "v_mul_f32 v146, v162, v146\n\t"
                "v_mul_f32 v147, v162, v147\n\t"
                "v_exp_f32 v160, v160\n\t"
                "v_cos_f32 v150, v161\n\t"
                "v_sin_f32 v151, v161\n\t"
                "v_fma_f32 v128, -%[ci], %[bi], v128\n\t"
                "v_fma_f32 v129, -%[ci], %[br], -v129\n\t"
                "v_mov_b32 v175, 0x3f80\n\t"
                "v_mul_f32 v150, v160, v150\n\t"
                "v_mul_f32 v151, v160, v151\n\t"
                "v_mul_f32 v128, %[dt], v128\n\t"
                "v_mul_f32 v129, %[dt], v129\n\t"
                "v_pk_mul_f32 v[152:153], v[148:149], v[148:149] op_sel_hi:[0,1]\n\t"
                "v_pk_mul_f32 v[154:155], v[128:129], v[146:147] op_sel_hi:[0,1]\n\t"
                "v_cvt_pk_bf16_f32 v168, v148, v149\n\t"
                "v_pk_fma_f32 v[144:145], v[148:149], v[148:149], v[152:153] op_sel:[1,1,0] op_sel_hi:[1,0,1] neg_lo:[0,1,0]\n\t"
                "v_pk_fma_f32 v[130:131], v[128:129], v[146:147], v[154:155] op_sel:[1,1,0] op_sel_hi:[1,0,1] neg_lo:[0,1,0]\n\t"
                "v_pk_mul_f32 v[152:153], v[144:145], v[148:149] op_sel_hi:[0,1]\n\t"
                "v_cvt_pk_bf16_f32 v169, v144, v145\n\t"
                "v_pk_fma_f32 v[144:145], v[144:145], v[148:149], v[152:153] op_sel:[1,1,0] op_sel_hi:[1,0,1] neg_lo:[0,1,0]\n\t"
                "s_nop 0\n\t"
                "v_pk_mul_f32 v[152:153], v[144:145], v[148:149] op_sel_hi:[0,1]\n\t"
                "v_pk_mul_f32 v[154:155], v[130:131], v[146:147] op_sel_hi:[0,1]\n\t"
                "v_cvt_pk_bf16_f32 v170, v144, v145\n\t"
                "v_pk_fma_f32 v[144:145], v[144:145], v[148:149], v[152:153] op_sel:[1,1,0] op_sel_hi:[1,0,1] neg_lo:[0,1,0]\n\t"
                "v_pk_fma_f32 v[132:133], v[130:131], v[146:147], v[154:155] op_sel:[1,1,0] op_sel_hi:[1,0,1] neg_lo:[0,1,0]\n\t"
                "ds_write_addtid_b32 v175 offset:0\n\t"
                "ds_write_addtid_b32 v168 offset:272\n\t"
                "ds_write_addtid_b32 v169 offset:544\n\t"
                "ds_write_addtid_b32 v170 offset:816\n\t"
                "v_pk_mul_f32 v[152:153], v[144:145], v[148:149] op_sel_hi:[0,1]\n\t"
                "v_cvt_pk_bf16_f32 v171, v144, v145\n\t"
                "v_pk_fma_f32 v[144:145], v[144:145], v[148:149], v[152:153] op_sel:[1,1,0] op_sel_hi:[1,0,1] neg_lo:[0,1,0]\n\t"
                "s_nop 0\n\t"
                "v_pk_mul_f32 v[152:153], v[144:145], v[148:149] op_sel_hi:[0,1]\n\t"
                "v_pk_mul_f32 v[154:155], v[132:133], v[146:147] op_sel_hi:[0,1]\n\t"
                "v_cvt_pk_bf16_f32 v172, v144, v145\n\t"
                "v_pk_fma_f32 v[144:145], v[144:145], v[148:149], v[152:153] op_sel:[1,1,0] op_sel_hi:[1,0,1] neg_lo:[0,1,0]\n\t"
                "v_pk_fma_f32 v[134:135], v[132:133], v[146:147], v[154:155] op_sel:[1,1,0] op_sel_hi:[1,0,1] neg_lo:[0,1,0]\n\t"
                "v_pk_mul_f32 v[152:153], v[144:145], v[148:149] op_sel_hi:[0,1]\n\t"
                "v_cvt_pk_bf16_f32 v173, v144, v145\n\t"
                "v_pk_fma_f32 v[144:145], v[144:145], v[148:149], v[152:153] op_sel:[1,1,0] op_sel_hi:[1,0,1] neg_lo:[0,1,0]\n\t"
                "s_nop 0\n\t"
                "v_pk_mul_f32 v[152:153], v[144:145], v[148:149] op_sel_hi:[0,1]\n\t"
                "v_pk_mul_f32 v[154:155], v[134:135], v[146:147] op_sel_hi:[0,1]\n\t"
                "v_cvt_pk_bf16_f32 v174, v144, v145\n\t"
                "v_pk_fma_f32 v[144:145], v[144:145], v[148:149], v[152:153] op_sel:[1,1,0] op_sel_hi:[1,0,1] neg_lo:[0,1,0]\n\t"
                "v_pk_fma_f32 v[136:137], v[134:135], v[146:147], v[154:155] op_sel:[1,1,0] op_sel_hi:[1,0,1] neg_lo:[0,1,0]\n\t"
                "ds_write_addtid_b32 v171 offset:1088\n\t"
                "ds_write_addtid_b32 v172 offset:1360\n\t"
                "ds_write_addtid_b32 v173 offset:1632\n\t"
                "ds_write_addtid_b32 v174 offset:1904\n\t"
                "v_pk_mul_f32 v[152:153], v[144:145], v[148:149] op_sel_hi:[0,1]\n\t"
                "v_cvt_pk_bf16_f32 v175, v144, v145\n\t"
                "v_pk_fma_f32 v[144:145], v[144:145], v[148:149], v[152:153] op_sel:[1,1,0] op_sel_hi:[1,0,1] neg_lo:[0,1,0]\n\t"
                "s_nop 0\n\t"
                "v_pk_mul_f32 v[152:153], v[144:145], v[148:149] op_sel_hi:[0,1]\n\t"
                "v_pk_mul_f32 v[154:155], v[136:137], v[146:147] op_sel_hi:[0,1]\n\t"
                "v_cvt_pk_bf16_f32 v168, v144, v145\n\t"
                "v_pk_fma_f32 v[144:145], v[144:145], v[148:149], v[152:153] op_sel:[1,1,0] op_sel_hi:[1,0,1] neg_lo:[0,1,0]\n\t"
                "v_pk_fma_f32 v[138:139], v[136:137], v[146:147], v[154:155] op_sel:[1,1,0] op_sel_hi:[1,0,1] neg_lo:[0,1,0]\n\t"
                "v_pk_mul_f32 v[152:153], v[144:145], v[148:149] op_sel_hi:[0,1]\n\t"
                "v_cvt_pk_bf16_f32 v169, v144, v145\n\t"
                "v_pk_fma_f32 v[144:145], v[144:145], v[148:149], v[152:153] op_sel:[1,1,0] op_sel_hi:[1,0,1] neg_lo:[0,1,0]\n\t"
                "s_nop 0\n\t"
                "v_pk_mul_f32 v[152:153], v[144:145], v[148:149] op_sel_hi:[0,1]\n\t"
                "v_pk_mul_f32 v[154:155], v[138:139], v[146:147] op_sel_hi:[0,1]\n\t"
                "v_cvt_pk_bf16_f32 v170, v144, v145\n\t"
                "v_pk_fma_f32 v[144:145], v[144:145], v[148:149], v[152:153] op_sel:[1,1,0] op_sel_hi:[1,0,1] neg_lo:[0,1,0]\n\t"
                "v_pk_fma_f32 v[140:141], v[138:139], v[146:147], v[154:155] op_sel:[1,1,0] op_sel_hi:[1,0,1] neg_lo:[0,1,0]\n\t"
                "ds_write_addtid_b32 v175 offset:2176\n\t"
                "ds_write_addtid_b32 v168 offset:2448\n\t"
                "ds_write_addtid_b32 v169 offset:2720\n\t"
                "ds_write_addtid_b32 v170 offset:2992\n\t"
                "v_pk_mul_f32 v[152:153], v[144:145], v[148:149] op_sel_hi:[0,1]\n\t"
                "v_cvt_pk_bf16_f32 v171, v144, v145\n\t"
                "v_pk_fma_f32 v[144:145], v[144:145], v[148:149], v[152:153] op_sel:[1,1,0] op_sel_hi:[1,0,1] neg_lo:[0,1,0]\n\t"
                "s_nop 0\n\t"
                "v_pk_mul_f32 v[152:153], v[144:145], v[148:149] op_sel_hi:[0,1]\n\t"
                "v_pk_mul_f32 v[154:155], v[140:141], v[146:147] op_sel_hi:[0,1]\n\t"
                "v_cvt_pk_bf16_f32 v172, v144, v145\n\t"
                "v_pk_fma_f32 v[144:145], v[144:145], v[148:149], v[152:153] op_sel:[1,1,0] op_sel_hi:[1,0,1] neg_lo:[0,1,0]\n\t"
                "v_pk_fma_f32 v[142:143], v[140:141], v[146:147], v[154:155] op_sel:[1,1,0] op_sel_hi:[1,0,1] neg_lo:[0,1,0]\n\t"
                "v_pk_mul_f32 v[152:153], v[144:145], v[148:149] op_sel_hi:[0,1]\n\t"
                "v_cvt_pk_bf16_f32 v173, v144, v145\n\t"
                "v_pk_fma_f32 v[144:145], v[144:145], v[148:149], v[152:153] op_sel:[1,1,0] op_sel_hi:[1,0,1] neg_lo:[0,1,0]\n\t"
                "s_nop 0\n\t"
                "v_cvt_pk_bf16_f32 v174, v144, v145\n\t"
                "ds_write_addtid_b32 v171 offset:3264\n\t"
                "ds_write_addtid_b32 v172 offset:3536\n\t"
                "ds_write_addtid_b32 v173 offset:3808\n\t"
                "ds_write_addtid_b32 v174 offset:4080\n\t"
                "ds_read_b128 v[64:67], %[fr] offset:0\n\t"
                "ds_read_b128 v[68:71], %[fr] offset:64\n\t"
                "ds_read_b128 v[72:75], %[fr] offset:128\n\t"
                "ds_read_b128 v[76:79], %[fr] offset:192\n\t"
                "s_cmp_lg_u32 %[rnd], 0\n\t"
                "s_cbranch_scc1 1f\n\t"
                "v_mov_b32 v160, 0\n\t"
                "v_mov_b64 v[162:163], %[tag]\n\t"
                "s_waitcnt vmcnt(0)\n\t"
                "s_mov_b64 exec, 1\n\t"
                "global_store_dwordx2 v160, v[162:163], %[fl] sc1\n\t"
                "s_mov_b64 exec, -1\n\t"
                "1:\n\t"
                "v_pk_mul_f32 v[152:153], v[128:129], v[150:151] op_sel_hi:[0,1]\n\t"
                "v_cvt_pk_bf16_f32 v175, v128, v129\n\t"
                "v_pk_fma_f32 v[128:129], v[128:129], v[150:151], v[152:153] op_sel:[1,1,0] op_sel_hi:[1,0,1] neg_lo:[0,1,0]\n\t"
                "v_pk_mul_f32 v[152:153], v[130:131], v[150:151] op_sel_hi:[0,1]\n\t"
                "v_cvt_pk_bf16_f32 v168, v130, v131\n\t"
                "v_pk_fma_f32 v[130:131], v[130:131], v[150:151], v[152:153] op_sel:[1,1,0] op_sel_hi:[1,0,1] neg_lo:[0,1,0]\n\t"
                "v_pk_mul_f32 v[152:153], v[132:133], v[150:151] op_sel_hi:[0,1]\n\t"
                "v_cvt_pk_bf16_f32 v169, v132, v133\n\t"
                "v_pk_fma_f32 v[132:133], v[132:133], v[150:151], v[152:153] op_sel:[1,1,0] op_sel_hi:[1,0,1] neg_lo:[0,1,0]\n\t"
                "v_pk_mul_f32 v[152:153], v[134:135], v[150:151] op_sel_hi:[0,1]\n\t"
                "v_cvt_pk_bf16_f32 v170, v134, v135\n\t"
                "v_pk_fma_f32 v[134:135], v[134:135], v[150:151], v[152:153] op_sel:[1,1,0] op_sel_hi:[1,0,1] neg_lo:[0,1,0]\n\t"
                "ds_write_addtid_b32 v175 offset:0\n\t"
                "ds_write_addtid_b32 v168 offset:272\n\t"
                "ds_write_addtid_b32 v169 offset:544\n\t"
                "ds_write_addtid_b32 v170 offset:816\n\t"
                "v_pk_mul_f32 v[152:153], v[136:137], v[150:151] op_sel_hi:[0,1]\n\t"
                "v_cvt_pk_bf16_f32 v171, v136, v137\n\t"
                "v_pk_fma_f32 v[136:137], v[136:137], v[150:151], v[152:153] op_sel:[1,1,0] op_sel_hi:[1,0,1] neg_lo:[0,1,0]\n\t"
                "v_pk_mul_f32 v[152:153], v[138:139], v[150:151] op_sel_hi:[0,1]\n\t"
                "v_cvt_pk_bf16_f32 v172, v138, v139\n\t"
                "v_pk_fma_f32 v[138:139], v[138:139], v[150:151], v[152:153] op_sel:[1,1,0] op_sel_hi:[1,0,1] neg_lo:[0,1,0]\n\t"
                "v_pk_mul_f32 v[152:153], v[140:141], v[150:151] op_sel_hi:[0,1]\n\t"
                "v_cvt_pk_bf16_f32 v173, v140, v141\n\t"
                "v_pk_fma_f32 v[140:141], v[140:141], v[150:151], v[152:153] op_sel:[1,1,0] op_sel_hi:[1,0,1] neg_lo:[0,1,0]\n\t"
                "v_pk_mul_f32 v[152:153], v[142:143], v[150:151] op_sel_hi:[0,1]\n\t"
                "v_cvt_pk_bf16_f32 v174, v142, v143\n\t"
                "v_pk_fma_f32 v[142:143], v[142:143], v[150:151], v[152:153] op_sel:[1,1,0] op_sel_hi:[1,0,1] neg_lo:[0,1,0]\n\t"
                "ds_write_addtid_b32 v171 offset:1088\n\t"
                "ds_write_addtid_b32 v172 offset:1360\n\t"
                "ds_write_addtid_b32 v173 offset:1632\n\t"
                "ds_write_addtid_b32 v174 offset:1904\n\t"
                "v_pk_mul_f32 v[152:153], v[128:129], v[150:151] op_sel_hi:[0,1]\n\t"
                "v_cvt_pk_bf16_f32 v175, v128, v129\n\t"
                "v_pk_fma_f32 v[128:129], v[128:129], v[150:151], v[152:153] op_sel:[1,1,0] op_sel_hi:[1,0,1] neg_lo:[0,1,0]\n\t"
                "v_cvt_pk_bf16_f32 v168, v130, v131\n\t"
                "v_pk_mul_f32 v[152:153], v[132:133], v[150:151] op_sel_hi:[0,1]\n\t"
                "v_cvt_pk_bf16_f32 v169, v132, v133\n\t"
                "v_pk_fma_f32 v[132:133], v[132:133], v[150:151], v[152:153] op_sel:[1,1,0] op_sel_hi:[1,0,1] neg_lo:[0,1,0]\n\t"
                "v_cvt_pk_bf16_f32 v170, v134, v135\n\t"
                "ds_write_addtid_b32 v175 offset:2176\n\t"
                "ds_write_addtid_b32 v168 offset:2448\n\t"
                "ds_write_addtid_b32 v169 offset:2720\n\t"
                "ds_write_addtid_b32 v170 offset:2992\n\t"
                "v_pk_mul_f32 v[152:153], v[136:137], v[150:151] op_sel_hi:[0,1]\n\t"
                "v_cvt_pk_bf16_f32 v171, v136, v137\n\t"
                "v_pk_fma_f32 v[136:137], v[136:137], v[150:151], v[152:153] op_sel:[1,1,0] op_sel_hi:[1,0,1] neg_lo:[0,1,0]\n\t"
                "v_cvt_pk_bf16_f32 v172, v138, v139\n\t"
                "v_pk_mul_f32 v[152:153], v[140:141], v[150:151] op_sel_hi:[0,1]\n\t"
                "v_cvt_pk_bf16_f32 v173, v140, v141\n\t"
                "v_pk_fma_f32 v[140:141], v[140:141], v[150:151], v[152:153] op_sel:[1,1,0] op_sel_hi:[1,0,1] neg_lo:[0,1,0]\n\t"
                "v_cvt_pk_bf16_f32 v174, v142, v143\n\t"
                "ds_write_addtid_b32 v171 offset:3264\n\t"
                "ds_write_addtid_b32 v172 offset:3536\n\t"
                "ds_write_addtid_b32 v173 offset:3808\n\t"
                "ds_write_addtid_b32 v174 offset:4080\n\t"
                "ds_read_b128 v[80:83], %[fr] offset:0\n\t"
                "ds_read_b128 v[84:87], %[fr] offset:64\n\t"
                "ds_read_b128 v[88:91], %[fr] offset:128\n\t"
                "ds_read_b128 v[92:95], %[fr] offset:192\n\t"
                "v_pk_mul_f32 v[152:153], v[128:129], v[150:151] op_sel_hi:[0,1]\n\t"
                "v_cvt_pk_bf16_f32 v175, v128, v129\n\t"
                "v_pk_fma_f32 v[128:129], v[128:129], v[150:151], v[152:153] op_sel:[1,1,0] op_sel_hi:[1,0,1] neg_lo:[0,1,0]\n\t"
                "v_pk_mul_f32 v[152:153], v[132:133], v[150:151] op_sel_hi:[0,1]\n\t"
                "v_cvt_pk_bf16_f32 v168, v132, v133\n\t"
                "v_pk_fma_f32 v[132:133], v[132:133], v[150:151], v[152:153] op_sel:[1,1,0] op_sel_hi:[1,0,1] neg_lo:[0,1,0]\n\t"
                "v_pk_mul_f32 v[152:153], v[136:137], v[150:151] op_sel_hi:[0,1]\n\t"
                "v_cvt_pk_bf16_f32 v169, v136, v137\n\t"
                "v_pk_fma_f32 v[136:137], v[136:137], v[150:151], v[152:153] op_sel:[1,1,0] op_sel_hi:[1,0,1] neg_lo:[0,1,0]\n\t"
                "v_pk_mul_f32 v[152:153], v[140:141], v[150:151] op_sel_hi:[0,1]\n\t"
                "v_cvt_pk_bf16_f32 v170, v140, v141\n\t"
                "v_pk_fma_f32 v[140:141], v[140:141], v[150:151], v[152:153] op_sel:[1,1,0] op_sel_hi:[1,0,1] neg_lo:[0,1,0]\n\t"
                "ds_write_addtid_b32 v175 offset:0\n\t"
                "ds_write_addtid_b32 v168 offset:272\n\t"
                "ds_write_addtid_b32 v169 offset:544\n\t"
                "ds_write_addtid_b32 v170 offset:816\n\t"
                "v_pk_mul_f32 v[152:153], v[128:129], v[150:151] op_sel_hi:[0,1]\n\t"
                "v_cvt_pk_bf16_f32 v171, v128, v129\n\t"
                "v_pk_fma_f32 v[128:129], v[128:129], v[150:151], v[152:153] op_sel:[1,1,0] op_sel_hi:[1,0,1] neg_lo:[0,1,0]\n\t"
                "v_cvt_pk_bf16_f32 v172, v132, v133\n\t"
                "v_pk_mul_f32 v[152:153], v[136:137], v[150:151] op_sel_hi:[0,1]\n\t"
                "v_cvt_pk_bf16_f32 v173, v136, v137\n\t"
                "v_pk_fma_f32 v[136:137], v[136:137], v[150:151], v[152:153] op_sel:[1,1,0] op_sel_hi:[1,0,1] neg_lo:[0,1,0]\n\t"
                "v_cvt_pk_bf16_f32 v174, v140, v141\n\t"
                "ds_write_addtid_b32 v171 offset:1088\n\t"
                "ds_write_addtid_b32 v172 offset:1360\n\t"
                "ds_write_addtid_b32 v173 offset:1632\n\t"
                "ds_write_addtid_b32 v174 offset:1904\n\t"
                "v_pk_mul_f32 v[152:153], v[128:129], v[150:151] op_sel_hi:[0,1]\n\t"
                "v_cvt_pk_bf16_f32 v175, v128, v129\n\t"
                "v_pk_fma_f32 v[128:129], v[128:129], v[150:151], v[152:153] op_sel:[1,1,0] op_sel_hi:[1,0,1] neg_lo:[0,1,0]\n\t"
                "v_pk_mul_f32 v[152:153], v[136:137], v[150:151] op_sel_hi:[0,1]\n\t"
                "v_cvt_pk_bf16_f32 v168, v136, v137\n\t"
                "v_pk_fma_f32 v[136:137], v[136:137], v[150:151], v[152:153] op_sel:[1,1,0] op_sel_hi:[1,0,1] neg_lo:[0,1,0]\n\t"
                "v_pk_mul_f32 v[152:153], v[128:129], v[150:151] op_sel_hi:[0,1]\n\t"
                "v_cvt_pk_bf16_f32 v169, v128, v129\n\t"
                "v_pk_fma_f32 v[128:129], v[128:129], v[150:151], v[152:153] op_sel:[1,1,0] op_sel_hi:[1,0,1] neg_lo:[0,1,0]\n\t"
                "v_pk_mul_f32 v[152:153], v[136:137], v[150:151] op_sel_hi:[0,1]\n\t"
                "v_cvt_pk_bf16_f32 v170, v136, v137\n\t"
                "v_pk_fma_f32 v[136:137], v[136:137], v[150:151], v[152:153] op_sel:[1,1,0] op_sel_hi:[1,0,1] neg_lo:[0,1,0]\n\t"
                "ds_write_addtid_b32 v175 offset:2176\n\t"
                "ds_write_addtid_b32 v168 offset:2448\n\t"
                "ds_write_addtid_b32 v169 offset:2720\n\t"
                "ds_write_addtid_b32 v170 offset:2992\n\t"
                "v_pk_mul_f32 v[152:153], v[128:129], v[150:151] op_sel_hi:[0,1]\n\t"
                "v_cvt_pk_bf16_f32 v171, v128, v129\n\t"
                "v_pk_fma_f32 v[128:129], v[128:129], v[150:151], v[152:153] op_sel:[1,1,0] op_sel_hi:[1,0,1] neg_lo:[0,1,0]\n\t"
                "v_pk_mul_f32 v[152:153], v[136:137], v[150:151] op_sel_hi:[0,1]\n\t"
                "v_cvt_pk_bf16_f32 v172, v136, v137\n\t"
                "v_pk_fma_f32 v[136:137], v[136:137], v[150:151], v[152:153] op_sel:[1,1,0] op_sel_hi:[1,0,1] neg_lo:[0,1,0]\n\t"
                "v_pk_mul_f32 v[152:153], v[128:129], v[150:151] op_sel_hi:[0,1]\n\t"
                "v_cvt_pk_bf16_f32 v173, v128, v129\n\t"
                "v_pk_fma_f32 v[128:129], v[128:129], v[150:151], v[152:153] op_sel:[1,1,0] op_sel_hi:[1,0,1] neg_lo:[0,1,0]\n\t"
                "v_cvt_pk_bf16_f32 v174, v136, v137\n\t"
                "ds_write_addtid_b32 v171 offset:3264\n\t"
                "ds_write_addtid_b32 v172 offset:3536\n\t"
                "ds_write_addtid_b32 v173 offset:3808\n\t"
                "ds_write_addtid_b32 v174 offset:4080\n\t"
                "ds_read_b128 v[96:99], %[fr] offset:0\n\t"
                "ds_read_b128 v[100:103], %[fr] offset:64\n\t"
                "ds_read_b128 v[104:107], %[fr] offset:128\n\t"
                "ds_read_b128 v[108:111], %[fr] offset:192\n\t"
                "v_pk_mul_f32 v[152:153], v[128:129], v[150:151] op_sel_hi:[0,1]\n\t"
                "v_cvt_pk_bf16_f32 v175, v128, v129\n\t"
                "v_pk_fma_f32 v[128:129], v[128:129], v[150:151], v[152:153] op_sel:[1,1,0] op_sel_hi:[1,0,1] neg_lo:[0,1,0]\n\t"
                "s_nop 0\n\t"
                "v_pk_mul_f32 v[152:153], v[128:129], v[150:151] op_sel_hi:[0,1]\n\t"
                "v_cvt_pk_bf16_f32 v168, v128, v129\n\t"
                "v_pk_fma_f32 v[128:129], v[128:129], v[150:151], v[152:153] op_sel:[1,1,0] op_sel_hi:[1,0,1] neg_lo:[0,1,0]\n\t"
                "s_nop 0\n\t"
                "v_pk_mul_f32 v[152:153], v[128:129], v[150:151] op_sel_hi:[0,1]\n\t"
                "v_cvt_pk_bf16_f32 v169, v128, v129\n\t"
                "v_pk_fma_f32 v[128:129], v[128:129], v[150:151], v[152:153] op_sel:[1,1,0] op_sel_hi:[1,0,1] neg_lo:[0,1,0]\n\t"
                "s_nop 0\n\t"
                "v_pk_mul_f32 v[152:153], v[128:129], v[150:151] op_sel_hi:[0,1]\n\t"
                "v_cvt_pk_bf16_f32 v170, v128, v129\n\t"
                "v_pk_fma_f32 v[128:129], v[128:129], v[150:151], v[152:153] op_sel:[1,1,0] op_sel_hi:[1,0,1] neg_lo:[0,1,0]\n\t"
                "ds_write_addtid_b32 v175 offset:0\n\t"
                "ds_write_addtid_b32 v168 offset:272\n\t"
                "ds_write_addtid_b32 v169 offset:544\n\t"
                "ds_write_addtid_b32 v170 offset:816\n\t"
                "v_pk_mul_f32 v[152:153], v[128:129], v[150:151] op_sel_hi:[0,1]\n\t"
                "v_cvt_pk_bf16_f32 v171, v128, v129\n\t"
                "v_pk_fma_f32 v[128:129], v[128:129], v[150:151], v[152:153] op_sel:[1,1,0] op_sel_hi:[1,0,1] neg_lo:[0,1,0]\n\t"
                "s_nop 0\n\t"
                "v_pk_mul_f32 v[152:153], v[128:129], v[150:151] op_sel_hi:[0,1]\n\t"
                "v_cvt_pk_bf16_f32 v172, v128, v129\n\t"
                "v_pk_fma_f32 v[128:129], v[128:129], v[150:151], v[152:153] op_sel:[1,1,0] op_sel_hi:[1,0,1] neg_lo:[0,1,0]\n\t"
                "s_nop 0\n\t"
                "v_pk_mul_f32 v[152:153], v[128:129], v[150:151] op_sel_hi:[0,1]\n\t"
                "v_cvt_pk_bf16_f32 v173, v128, v129\n\t"
                "v_pk_fma_f32 v[128:129], v[128:129], v[150:151], v[152:153] op_sel:[1,1,0] op_sel_hi:[1,0,1] neg_lo:[0,1,0]\n\t"
                "s_nop 0\n\t"
                "v_pk_mul_f32 v[152:153], v[128:129], v[150:151] op_sel_hi:[0,1]\n\t"
                "v_cvt_pk_bf16_f32 v174, v128, v129\n\t"
                "v_pk_fma_f32 v[128:129], v[128:129], v[150:151], v[152:153] op_sel:[1,1,0] op_sel_hi:[1,0,1] neg_lo:[0,1,0]\n\t"
                "ds_write_addtid_b32 v171 offset:1088\n\t"
                "ds_write_addtid_b32 v172 offset:1360\n\t"
                "ds_write_addtid_b32 v173 offset:1632\n\t"
                "ds_write_addtid_b32 v174 offset:1904\n\t"
                "v_cvt_pk_bf16_f32 v175, v128, v129\n\t"
                "ds_write_addtid_b32 v175 offset:2176\n\t"
                "ds_read_b128 v[112:115], %[fr] offset:0\n\t"
                "ds_read_b128 v[116:119], %[fr] offset:64\n\t"
                "ds_read_b128 v[120:123], %[fr] offset:128\n\t"
                "ds_read_b128 v[124:127], %[fr] offset:192\n\t"
                "s_waitcnt lgkmcnt(4)\n\t"
                "v_mfma_f32_16x16x32_bf16 %[k0], v[80:83], v[64:67], %[k0]\n\t"
                "v_mfma_f32_16x16x32_bf16 %[k0], v[84:87], v[68:71], %[k0]\n\t"
                "v_mfma_f32_16x16x32_bf16 %[k0], v[88:91], v[72:75], %[k0]\n\t"
                "v_mfma_f32_16x16x32_bf16 %[k0], v[92:95], v[76:79], %[k0]\n\t"
                "v_mfma_f32_16x16x32_bf16 %[k1], v[96:99], v[64:67], %[k1]\n\t"
                "v_mfma_f32_16x16x32_bf16 %[k1], v[100:103], v[68:71], %[k1]\n\t"
                "v_mfma_f32_16x16x32_bf16 %[k1], v[104:107], v[72:75], %[k1]\n\t"
                "v_mfma_f32_16x16x32_bf16 %[k1], v[108:111], v[76:79], %[k1]\n\t"
                "s_waitcnt lgkmcnt(0)\n\t"
                "v_mfma_f32_16x16x32_bf16 %[k2], v[112:115], v[64:67], %[k2]\n\t"
                "v_mfma_f32_16x16x32_bf16 %[k2], v[116:119], v[68:71], %[k2]\n\t"
                "v_mfma_f32_16x16x32_bf16 %[k2], v[120:123], v[72:75], %[k2]\n\t"
                "v_mfma_f32_16x16x32_bf16 %[k2], v[124:127], v[76:79], %[k2]\n\t"
                "s_nop 7\n\t"
                    : [k0] "+v"(ka[0]), [k1] "+v"(ka[1]), [k2] "+v"(ka[2])
                    : [dt] "v"(dt), [ar] "v"(pa_[0][0]), [ai] "v"(pa_[0][1]), [br] "v"(pa_[0][2]), [bi] "v"(pa_[0][3]), [cr] "v"(pa_[0][4]), [ci] "v"(pa_[0][5]), [fr] "v"(fr),
                      [rnd] "s"(r), [tag] "s"(tag), [fl] "s"(ws + WS_FT + ((tc * 4 + tw) * 32 + trb) * 128)
                    : "memory", "scc", "v64", "v65", "v66", "v67", "v68", "v69", "v70", "v71", "v72", "v73", "v74", "v75", "v76", "v77", "v78", "v79", "v80", "v81", "v82", "v83", "v84", "v85", "v86", "v87", "v88", "v89", "v90", "v91", "v92", "v93", "v94", "v95", "v96", "v97", "v98", "v99", "v100", "v101", "v102", "v103", "v104", "v105", "v106", "v107", "v108", "v109", "v110", "v111", "v112", "v113", "v114", "v115", "v116", "v117", "v118", "v119", "v120", "v121", "v122", "v123", "v124", "v125", "v126", "v127", "v128", "v129", "v130", "v131", "v132", "v133", "v134", "v135", "v136", "v137", "v138", "v139", "v140", "v141", "v142", "v143", "v144", "v145", "v146", "v147", "v148", "v149", "v150", "v151", "v152", "v153", "v154", "v155", "v156", "v157", "v158", "v159", "v160", "v161", "v162", "v163", "v164", "v165", "v166", "v167", "v168", "v169", "v170", "v171", "v172", "v173", "v174", "v175");
#pragma unroll
                for (int i = 0; i < 6; ++i) pa_[0][i] = pa_[1][i];
            }
            STAMP(2);
            __builtin_amdgcn_s_setprio(CONV_PRIO);
            {
                LAS char* const t = scr;
                const int i = lane & 15, g = lane >> 4;
                *(LAS f32x4*)(t + i * 32 + (g & 1) * 16 + (g >> 1) * 512) = ka[0];
                *(LAS f32x2*)(t + 1024 + g * 256 + (hh ? i * 8 : i * 16)) = (f32x2){ka[1][0], ka[1][1]};
                *(LAS f32x2*)(t + 1024 + g * 256 + (hh ? i * 8 + 128 : i * 16 + 8)) = (f32x2){ka[1][2], ka[1][3]};
#pragma unroll
                for (int a = 0; a < 4; ++a) *(LAS float*)(t + 2048 + g * 256 + a * 64 + i * 4) = ka[2][a];
            }
            arrive(pc);
            wait_ge(pc, 2u);
            {
                float t0_[16], t1_[16];
#pragma unroll
                for (int e = 0; e < 16; ++e) { t0_[e] = *(LAS float*)(uintptr_t)(pre[e] + B_T); t1_[e] = *(LAS float*)(uintptr_t)(pre[e] + B_T + B_SCR); }
                set_m0((u32)(uintptr_t)cb_ + B_KR + pw * 2048);
                SBAR();
                static_for<0, 8>([&](auto kc) { constexpr int k = decltype(kc)::value;
                    put_tid<k * 256>(pk_bf16(nadd(t0_[2 * k], t1_[2 * k]), nadd(t0_[2 * k + 1], t1_[2 * k + 1]))); });
            }
            arrive(pc);
            f32x4 ya[8] = {};
#pragma unroll
            for (int c = 0; c < 8; ++c) PIN(ya[c]);
            {
                const int p = (lane & 15) + 8 * (lane >> 4) + 1, sg = (-p) & 3;
                LAS char* pa = cb_ + B_BIG + sg * B_UC + (p + sg) * 2 + pw * 256;
                LAS char* pb = cb_ + B_KR + (254 - 2 * (lane & 15) + (lane >> 4) - 224) * 16 + pw * 256;
                u32x2 fa[2][8][2]; u32x4 fb[2][8];
                STAMPX(4);
                wait_ge(pc, 4u); wait_ge(ctl + C_XT + (tw >> 1) * 28, 2u);
                STAMPX(3);
#define CRD(s, r, d) do { constexpr int i_ = (r) / 3; if constexpr ((r) % 3 == 0) fb[s][i_] = lds128(pb + (7 - i_) * 512 + (d)); \
                          else fa[s][i_][(r) % 3 - 1] = *(volatile LAS u32x2*)(pa + i_ * 512 + ((r) % 3 - 1) * 8 + (d)); } while (0)
                static_for<0, 24>([&](auto rc) { CRD(0, decltype(rc)::value, 0); });
#pragma nounroll
                for (int mu = 0; mu < 2; ++mu) {
                    static_for<0, 72>([&](auto mc) {
                        constexpr int m = decltype(mc)::value % 36, s = decltype(mc)::value / 36;
                        constexpr int c = m < 1 ? 0 : m < 3 ? 1 : m < 6 ? 2 : m < 10 ? 3 : m < 15 ? 4 : m < 21 ? 5 : m < 28 ? 6 : 7, i = m - c * (c + 1) / 2;
                        SBAR();
                        ya[c] = MFMA16(cat(fa[s][i][0], fa[s][i][1]), fb[s][c - i], ya[c]); PIN(ya[c]);
                        SBAR();
                        if constexpr (m >= 8 && m < 32) CRD(s ^ 1, m - 8, 64 * (s + 1));
                    });
                    pa += 128; pb += 128;
                }
            }
            STAMPX(5);
            f32x4 keep[4];
            if (pw) {
#pragma unroll
                for (int c = 0; c < 4; ++c) { *(LAS f32x4*)(cb_ + B_T + 4096 + c * 1024 + lane * 16) = ya[c]; keep[c] = ya[4 + c]; }
            } else {
#pragma unroll
                for (int c = 0; c < 4; ++c) { *(LAS f32x4*)(cb_ + B_T + c * 1024 + lane * 16) = ya[4 + c]; keep[c] = ya[c]; }
            }
            arrive(pc); wait_ge(pc, 6u);
            f32x4 oth[4];
#pragma unroll
            for (int c = 0; c < 4; ++c) oth[c] = *(LAS f32x4*)(cb_ + B_T + (pw ^ 1) * 4096 + c * 1024 + lane * 16);
#pragma unroll
            for (int c = 0; c < 4; ++c)
                yq[c] = (u32x2){pk_bf16(nadd(keep[c][0], oth[c][0]), nadd(keep[c][1], oth[c][1])), pk_bf16(nadd(keep[c][2], oth[c][2]), nadd(keep[c][3], oth[c][3]))};
            yo = WS_YT + (u32)(ch * 4096 + pw * 2048 + (lane & 15) * 32 + (lane >> 4) * 8);
        }
    }
    if (team) { arrive(ctl + C_L); att_setup(); }
    STAMP(6);

    const int qs = WS_QK + G8 * 512, ks_ = qs + 56 * 512, vs = WS_V + head * 2048;
#define LDQ(p) do { const int o_ = qs + (qt0 + ((tw + (p)) & 3)) * QK_TILE; qf[p][0] = LDC128(l16, o_); qf[p][1] = LDC128(l16 + 1024, o_); } while (0)
#define LDK(t) do { const int o_ = ks_ + (8 * wid + (t)) * QK_TILE; kf[t][0] = LDC128(l16, o_); kf[t][1] = LDC128(l16 + 1024, o_); } while (0)
#define LDV(t) do { const int o_ = vs + (8 * wid + (t)) * V_TILE; vf[t][0] = LDC128(l16, o_); vf[t][1] = LDC128(l16 + 1024, o_); } while (0)
#define FIXQ(p) do { qf[p][0][0] &= m0_; qf[p][0][1] &= m0_; qf[p][1][2] &= m1_; qf[p][1][3] &= m1_; } while (0)
#define GETQ(p) do { LAS char* const q_ = sm + L_Q + ((tw + (p)) & 3) * 2048 + l16; qf[p][0] = lds128(q_); qf[p][1] = lds128(q_ + 1024); } while (0)
#define PUTO(p, i) *(LAS f32x4*)(sm + part + (p) * 4096 + (i) * 1024) = (f32x4){O[p][4 * (i)], O[p][4 * (i) + 1], O[p][4 * (i) + 2], O[p][4 * (i) + 3]}
#define GETO(p) do { _Pragma("unroll") for (int i_ = 0; i_ < 4; ++i_) { const f32x4 v_ = *(volatile LAS f32x4*)(sm + from + (TB ? (p) * 4096 + i_ * 1024 : 0)); \
                     O[p][4 * i_] = v_[0]; O[p][4 * i_ + 1] = v_[1]; O[p][4 * i_ + 2] = v_[2]; O[p][4 * i_ + 3] = v_[3]; } } while (0)
#define PINS(x, k) do { if constexpr ((k) == 0) asm volatile("" : "+{v[0:15]}"(x)); else if constexpr ((k) == 1) asm volatile("" : "+{v[16:31]}"(x)); else asm volatile("" : "+{v[32:47]}"(x)); } while (0)
#define PINO(x, k) do { if constexpr (((k) & 1) == 0) asm volatile("" : "+{v[48:63]}"(x)); else asm volatile("" : "+{v[64:79]}"(x)); } while (0)
#define LO8(S) __builtin_shufflevector(S, S, 0, 1, 2, 3, 4, 5, 6, 7)
#define HI8(S) __builtin_shufflevector(S, S, 8, 9, 10, 11, 12, 13, 14, 15)
#define EXP4(S, i) do { SBAR(); S[i] = __builtin_amdgcn_exp2f(S[i]); S[i + 1] = __builtin_amdgcn_exp2f(S[i + 1]); S[i + 2] = __builtin_amdgcn_exp2f(S[i + 2]); S[i + 3] = __builtin_amdgcn_exp2f(S[i + 3]); PINS(S, n % 3); SBAR(); } while (0)
#define POLL_UNTIL(need) do { _Pragma("nounroll") for (u32 sp = 0; sp < SPIN_CAP; ++sp) { \
            bool seen_ = true; if ((todo >> (l16 >> 4)) & 1) seen_ = get_flag(ws, fo) == tag; \
            todo = __ballot(!seen_); if ((todo & (need)) == 0) break; \
            TMO(sp, 0); __builtin_amdgcn_s_sleep(POLL_SLEEP); } asm volatile("" ::: "memory"); } while (0)
#define B_DELAY 10
    const f32x16 zero16 = {};
    LAS char* const hc = ctl + C_H0 + tw * 4;
    const int q3 = (tw + 3) & 3;
    const int k1 = tw < 3 ? 1 : 0;
    auto gather = [&]() {
        u32x4 a[4];
        const int oo = WS_O + rt * 32768 + wid * 4096;
#pragma nounroll
        for (u32 sp = 0; sp < SPIN_CAP; ++sp) {
            u32 mx = 0;
#pragma unroll
            for (int i = 0; i < 4; ++i) a[i] = LDC128(l16 + i * 1024, oo);
#pragma unroll
            for (int i = 0; i < 4; ++i) mx = max(max(mx, max(a[i][0], a[i][1])), max(a[i][2], a[i][3]));
            if (__all(mx != 0xffffffffu)) break;
            TMO(sp, 3);
            asm volatile("s_sleep " OSLEEP ::: "memory");
        }
#pragma unroll
        for (int i = 0; i < 4; ++i) *(LAS u32x4*)(sm + L_O2 + wid * 4096 + i * 1024 + l16) = a[i];
        STAMPNQ(11);
        asm volatile("s_waitcnt lgkmcnt(0)\n\ts_barrier" ::: "memory");
    };
    auto attend = [&](auto tb) {
        constexpr bool TB = decltype(tb)::value;
        u32x4 qf[4][2], kf[8][2], vf[8][2], wof[2][16];
        f32x16 O[4], S[3];
        f32x4 pv[4];
        u64 todo = ~0ull;
        u32 hv = 0;
        LAS char* const ps = sm + tw * 4096 + l16;
        if constexpr (TB) {
            POLL_UNTIL(~0ull);
            wait_ge(ctl + C_KV, 4u);
            STAMP(7);
#pragma unroll
            for (int t = 0; t < B_PRE; ++t) { LDK(t); LDV(t); }
            wait_ge(ctl + C_L, 4u);
            GETQ(0); GETQ(1);
            wait_ge(hc, 1u);
            GETO(0); GETO(1);
            SBAR();
            S[0] = MFMA32(kf[0][0], qf[0][0], zero16); S[0] = MFMA32(kf[0][1], qf[0][1], S[0]);
            __builtin_amdgcn_s_setprio(BP_END ? 1 : 0);
        } else {
            POLL_UNTIL(0x0000003307070707ull);
            STAMP(7);
            LDQ(0); LDK(0); LDQ(1);
            SBAR();
            GETO(0); GETO(1);
            SBAR();
            FIXQ(0);
            S[0] = MFMA32(kf[0][0], qf[0][0], zero16); S[0] = MFMA32(kf[0][1], qf[0][1], S[0]);
            LDK(1); LDV(0);
            *(LAS u32x4*)(sm + L_Q + tw * 2048 + l16) = qf[0][0]; *(LAS u32x4*)(sm + L_Q + tw * 2048 + 1024 + l16) = qf[0][1];
            FIXQ(1);
            __builtin_amdgcn_s_setprio(0);
        }
        STAMPNQ(8);
        static_for<0, 33>([&](auto ic) {
            constexpr int n = decltype(ic)::value;
            constexpr int n1 = n + 1, P1 = n1 < 16 ? (n1 & 1) : (n1 >> 3), T1 = n1 < 16 ? (n1 >> 1) : (n1 & 7);
            constexpr int n0 = n > 0 ? n - 1 : 0, P0 = n0 < 16 ? (n0 & 1) : (n0 >> 3), T0 = n0 < 16 ? (n0 >> 1) : (n0 & 7);
#define LDWO(i) wof[(i) >> 4][(i) & 15] = LDC128(l16 + ((i) & 3) * 1024, WS_WOF + (7 * cq + 2 * tw + ((i) >> 4) * k1) * 16384 + (((i) >> 2) & 3) * 4096)
            f32x16& C = S[n % 3]; f32x16& N = S[(n + 1) % 3]; f32x16& L = S[(n + 2) % 3];
            if constexpr (n < 31) { N = MFMA32(kf[T1][0], qf[P1 & 3][0], zero16); PINS(N, (n + 1) % 3); }
            if constexpr (!TB) {
                if constexpr (n == 1) { if (__builtin_expect(todo != 0, 0)) POLL_UNTIL(~0ull); }
                if constexpr (n == 0) LDK(2);
                if constexpr (n >= 1 && n < 11) { if constexpr (n & 1) LDK((n + 5) / 2); else LDV((n + 4) / 2); }
                if constexpr (n == B_DELAY) arrive(ctl + C_KV);
                if constexpr (n == 13) hv = *(volatile LAS u32*)(ctl + C_KV);
                if constexpr (n >= 26 && n < 32) { LDWO(4 * (n - 26)); LDWO(4 * (n - 26) + 1); }
            } else {
                if constexpr (n == BP_END && n > 0) __builtin_amdgcn_s_setprio(0);
                if constexpr (n + B_PRE < 8) { LDK(n + B_PRE); LDV(n + B_PRE); }
                if constexpr (n + B_PRE == 9)
            {
                const char* const xs = x + rt * (32 * 896) + tw * 7168;
                set_m0(sm0 + (tw < 2 ? L_XR0 : L_XR1 - 14336) + tw * 7168 + 3072);
                dma16<-3072>(l16, xs + 3072); dma16<-2048>(l16, xs + 3072); dma16<-1024>(l16, xs + 3072); dma16<0>(l16, xs + 3072);
                dma16<1024>(l16, xs + 3072); dma16<2048>(l16, xs + 3072); dma16<3072>(l16, xs + 3072);
                set_m0(sm0 + L_GW + (tw & 1) * 1024);
                if (tw < 2 && l16 < 896u) dma16<0>(l16, tw ? Dskip : gatew);
            }
                if constexpr (n + B_PRE == 11)
            {
#pragma unroll
                for (int c = 0; c < 4; ++c) __builtin_amdgcn_raw_buffer_store_b64(yq[c], rs, yo + c * 512, 0, 16);
                const u32x4 ones = {~0u, ~0u, ~0u, ~0u};
                const u32 o3 = WS_O + (u32)((qt0 + tw) * 32768 + head * 2048) + l16;
                STC128(ones, o3); STC128(ones, o3 + 1024);
            }
                if constexpr (n == FY_STEP) {
                    VMCNT(0);
                    if (arrive_rtn(ctl + C_Y) == 3u) put_flag(ws, WS_FY + blockIdx.x * 8, tag, l16);
                }
            }
            if constexpr (n < 32) EXP4(C, 0);
            if constexpr (n < 31) { N = MFMA32(kf[T1][1], qf[P1 & 3][1], N); PINS(N, (n + 1) % 3); }
            if constexpr (!TB) {
                if constexpr (n < 2) LDV(n + 1);
                if constexpr (n == 14) { if (__builtin_expect((u32)rfl((int)hv) < 4u, 0)) wait_ge(ctl + C_KV, 4u); }
                if constexpr (n >= 26 && n < 32) { LDWO(4 * (n - 26) + 2); LDWO(4 * (n - 26) + 3); }
            } else if constexpr (n == 15 || n == 23 || n == 30) hv = *(volatile LAS u32*)hc;
            if constexpr (n == 14) GETQ(2);
            if constexpr (n == 22) GETQ(3);
            if constexpr (n == 16) { PUTO(0, 0); PUTO(0, 1); }
            if constexpr (n == 17) { PUTO(1, 0); PUTO(1, 1); }
            if constexpr (n == 25) { PUTO(2, 0); PUTO(2, 1); }
            if constexpr (n < 32) EXP4(C, 4);
            if constexpr (n > 0) { O[P0] = SMFMAC(vf[T0][0], LO8(L), O[P0], ix); PINO(O[P0], P0); }
            if constexpr (n == 16) { PUTO(0, 2); PUTO(0, 3); }
            if constexpr (n == 17) { PUTO(1, 2); PUTO(1, 3); if constexpr (!TB) arrive(hc); }
            if constexpr (n == 25) { PUTO(2, 2); PUTO(2, 3); arrive(TB ? ctl + C_M : hc); }
            if constexpr (n < 32) EXP4(C, 8);
            if constexpr (n > 0) { O[P0] = SMFMAC(vf[T0][1], HI8(L), O[P0], ix); PINO(O[P0], P0); }
            if constexpr (n == 16 || n == 24) {
                if constexpr (TB) if (__builtin_expect((u32)rfl((int)hv) < (u32)(n >> 3), 0)) wait_ge(hc, (u32)(n >> 3));
                GETO(n >> 3);
            }
            if constexpr (TB && n == 31) {
                if (__builtin_expect((u32)rfl((int)hv) < 4u, 0)) wait_ge(hc, 4u);
#pragma unroll
                for (int i = 0; i < 4; ++i) pv[i] = *(LAS f32x4*)(ps + i * 1024);
            }
            if constexpr (n < 32) EXP4(C, 12);
        });
        STAMPNQ(9);
        PIN(l16);
        const int ln = l16 >> 4;
        if constexpr (!TB) {
            PUTO(3, 0); PUTO(3, 1); PUTO(3, 2); PUTO(3, 3);
            arrive(hc);
            if (tw == 0) {
                POLL256(WS_FY + l16, 2);
                if (blockIdx.x == 0) put_flag(ws, WS_EPOCH, tag, ln);
                arrive(ctl + C_FY);
            }
            static_for<24, 32>([&](auto ic) { LDWO(decltype(ic)::value); });
            u32x2 dk[2], xv[2][2]; u32 yv[2][2][4];
            const int c1 = 112 * cq + 32 * tw + 4 * (ln >> 4), t1 = 32 * rt + (ln & 15);
            wait_ge(ctl + C_FY, 1u);
#pragma unroll
            for (int k = 0; k < 2; ++k)
#pragma unroll
                for (int r = 0; r < 2; ++r)
#pragma unroll
                    for (int e = 0; e < 4; ++e) yv[k][r][e] = __builtin_amdgcn_raw_buffer_load_b16(rs, (u32)(c1 * 4096 + t1 * 2 + 32 * r), WS_YT + (16 * k * k1 + e) * 4096, 16);
            wait_ge(ctl + C_M, 4u);
#pragma unroll
            for (int i2 = 0; i2 < 4; i2 += 2) {
                f32x4 r[3][2];
#pragma unroll
                for (int w = 0; w < 3; ++w)
#pragma unroll
                    for (int i = 0; i < 2; ++i) r[w][i] = *(LAS f32x4*)(sm + L_A + ((tw + 1 + w) & 3) * 16384 + (2 - w) * 4096 + (i2 + i) * 1024 + l16);
                SBAR();
#pragma unroll
                for (int i = 0; i < 2; ++i) {
                    f32x4 v;
#pragma unroll
                    for (int e = 0; e < 4; ++e) v[e] = nadd(nadd(r[0][i][e], r[1][i][e]), r[2][i][e]);
                    *(LAS f32x4*)(ps + (i2 + i) * 1024) = v;
                }
                SBAR();
            }
            arrive(hc);
#pragma unroll
            for (int k = 0; k < 2; ++k) {
                dk[k] = *(LAS u32x2*)(sm + L_DK + (c1 + 16 * k * k1) * 2);
#pragma unroll
                for (int r = 0; r < 2; ++r) xv[k][r] = *(LAS u32x2*)(sm + (r ? L_XR1 : L_XR0) + (ln & 15) * 896 + (c1 + 16 * k * k1) * 2);
            }
            wait_ge(hc, 5u);
            STAMPNQ(10);
        float bt[2][2][4];
#pragma unroll
        for (int k = 0; k < 2; ++k)
#pragma unroll
            for (int r = 0; r < 2; ++r)
#pragma unroll
                for (int e = 0; e < 4; ++e) {
                    const float xe = (e & 1) ? bf_hi(xv[k][r][e >> 1]) : bf_lo(xv[k][r][e >> 1]), de = (e & 1) ? bf_hi(dk[k][e >> 1]) : bf_lo(dk[k][e >> 1]);
                    bt[k][r][e] = bf_lo(yv[k][r][e]) + xe * de;
                    PIN(bt[k][r][e]);
                }
        u32 op = (u32)(t1 * 896 + c1 * 2); PIN(op);
        __builtin_amdgcn_s_sleep(O_FIRST);
        gather();
        f32x4 oa[2][2] = {};
        LAS char* const p = sm + L_O2 + (ln >> 4) * 512 + (ln & 15) * 16;
#define OFA(h) (((h) >> 1) * 2048 + ((h) & 1) * 256)
        u32x4 of[32]; float gg[2];
#pragma unroll
        for (int h = 0; h < 6; ++h) of[h] = lds128(p + OFA(h));
        static_for<0, 32>([&](auto hc_) {
            constexpr int h = decltype(hc_)::value;
            SBAR();
            oa[0][h & 1] = MFMA16(wof[0][h >> 1], of[h], oa[0][h & 1]);
            SBAR();
            if constexpr (h + 6 < 32) of[h + 6] = lds128(p + OFA(h + 6));
            if constexpr (h == 26 || h == 27) gg[h - 26] = *(LAS float*)(ctl + C_G + (16 * (h - 26) + (ln & 15)) * 4);
            SBAR();
            oa[1][h & 1] = MFMA16(wof[1][h >> 1], of[h], oa[1][h & 1]);
            SBAR();
        });
#pragma unroll
        for (int k = 0; k < 2; ++k)
#pragma unroll
            for (int r = 0; r < 2; ++r) {
                float v[4];
#pragma unroll
                for (int e = 0; e < 4; ++e) v[e] = bt[k][r][e] + gg[r] * (oa[k][r][e] - bt[k][r][e]);
                *(u32x2*)(out + r * (16 * 896) + k * k1 * 32 + op) = (u32x2){pk_bf16(v[0], v[1]), pk_bf16(v[2], v[3])};
            }
        } else {
            f32x16 o = O[3];
#pragma unroll
            for (int i = 0; i < 4; ++i)
#pragma unroll
                for (int e = 0; e < 4; ++e) o[4 * i + e] += pv[i][e];
        float l = __uint_as_float(__builtin_amdgcn_permlane32_swap(__float_as_uint(o[12]), __float_as_uint(o[12]), false, false)[1]);
        float mo = 0.f;
#pragma unroll
        for (int e = 0; e < 16; e += 2) mo = fmaxf(fmaxf(mo, fabsf(o[e])), fabsf(o[e + 1]));
        if (__builtin_expect(__any(!(l < 1.0e18f && l > 1.0e-12f && mo < 3.0e38f)), 0)) {
            float m = -INFINITY;
#pragma nounroll
            for (int t = 0; t < 128; ++t) {
                f32x16 s = MFMA32(LDC128(l16, ks_ + (t & 63) * QK_TILE), qf[3][0], zero16);
                s = MFMA32(LDC128(l16 + 1024, ks_ + (t & 63) * QK_TILE), qf[3][1], s);
                if (t < 64) {
#pragma unroll
                    for (int r = 0; r < 16; ++r) m = fmaxf(m, s[r]);
                    if (t == 63) { m = fmaxf(m, __shfl_xor(m, 32)); o = zero16; }
                } else {
                    const u32x4 a = LDC128(l16, vs + (t & 63) * V_TILE), b = LDC128(l16 + 1024, vs + (t & 63) * V_TILE);
#pragma unroll
                    for (int r = 0; r < 16; ++r) s[r] = __builtin_amdgcn_exp2f(s[r] - m);
                    o = SMFMAC(a, LO8(s), o, ix); o = SMFMAC(b, HI8(s), o, ix);
                }
            }
            l = __shfl(o[12], 32 + (ln & 31));
        }
        const float rl = __builtin_amdgcn_rcpf(l);
#pragma unroll
        for (int s = 0; s < 2; ++s) {
            u32x4 v;
#pragma unroll
            for (int e = 0; e < 4; ++e) v[e] = min(pk_bf16(o[8 * s + 2 * e] * rl, o[8 * s + 2 * e + 1] * rl), 0xfffffffeu);
            STC128(v, WS_O + (u32)((qt0 + q3) * 32768 + head * 2048 + s * 1024) + l16);
        }
        arrive(hc);
        STAMPNQ(10);
        {
            u32x4 ga[7], gw[7];
            LAS char* const gx = sm + (tw < 2 ? L_XR0 : L_XR1 - 14336) + (8 * tw + (ln >> 3)) * 896 + (ln & 7) * 16;
#pragma unroll
            for (int i = 0; i < 7; ++i) { ga[i] = lds128(gx + i * 128); gw[i] = lds128(sm + L_GW + (ln & 7) * 16 + i * 128); }
            SBAR();
            float s = 0.f;
#pragma unroll
            for (int i = 0; i < 7; ++i)
#pragma unroll
                for (int e = 0; e < 4; ++e) s = dot2(ga[i][e], gw[i][e], s);
            s += __builtin_bit_cast(float, __builtin_amdgcn_update_dpp(0, __builtin_bit_cast(int, s), 0xB1, 0xf, 0xf, true));
            s += __builtin_bit_cast(float, __builtin_amdgcn_update_dpp(0, __builtin_bit_cast(int, s), 0x4E, 0xf, 0xf, true));
            s += __builtin_bit_cast(float, __builtin_amdgcn_update_dpp(0, __builtin_bit_cast(int, s), 0x141, 0xf, 0xf, true));
            *(LAS float*)(ctl + C_G + (8 * tw + (ln >> 3)) * 4) = __builtin_amdgcn_rcpf(1.f + __builtin_amdgcn_exp2f(-1.4426950408889634f * s));
        }
        gather();
        }
    };
    if (team) attend(BoolC<true>{}); else attend(BoolC<false>{});
    asm volatile(".Lcode_end:" ::: "memory");
    STAMP(12);
}

extern "C" void kernel_launch(void* const* d_in, const int* in_sizes, int n_in,
                              void* d_out, int out_size, void* d_ws, size_t ws_size,
                              hipStream_t stream) {
    Tab tab;
    for (int b = 0; b < 256; ++b) {
        const int xcd = b & 7, j = b >> 3;
        int rb, cb, qv = j < 30;
        if (xcd < 3) { rb = 6 * xcd + j / 5; cb = j % 5; }
        else if (xcd == 3) { qv = 1; rb = 18 + (j >> 3); cb = j & 7; }
        else if (xcd < 7 || j < 18) { rb = 5 * (xcd - 4) + j / 6; cb = 5 + j % 6; }
        else { rb = 18 + (j - 18) / 3; cb = 8 + (j - 18) % 3; }
        const int t0 = rb == 21 ? 61 : 3 * rb, ct0 = cb == 10 ? 38 : 4 * cb;
        tab.e[b] = qv ? (u32)(t0 | (ct0 << 8) | (rb << 16)) | 0x80000000u : (u32)((2 * (xcd < 3 ? xcd : xcd - 1) + (j & 1)) << 16);
    }
    fused_kernel<<<256, 512, 0, stream>>>((const char*)d_in[0], (const char*)d_in[1], (const char*)d_in[2],
        (const char*)d_in[4], (const char*)d_in[5], (const char*)d_in[6], (const char*)d_in[7], (const char*)d_in[8], (const char*)d_in[9],
        (const char*)d_in[10], (const char*)d_in[11], (const char*)d_in[12], (char*)d_out, (char*)d_ws, tab);
}
```
